```python
import torch

def _softmax(x, axis=-1):
    return torch.softmax(x, dim=axis)
def _gelu(x):
    return torch.nn.functional.gelu(x, approximate="tanh")
def _silu(x):
    return torch.nn.functional.silu(x)
def _sigmoid(x):
    return torch.sigmoid(x)
def _layernorm(x, axis=-1, eps=1e-5):
    m = x.mean(dim=axis, keepdim=True)
    v = x.var(dim=axis, keepdim=True, unbiased=False)
    return (x - m) / torch.sqrt(v + eps)
def _scatter_add(tgt, idx, src, axis=0):
    return tgt.clone().index_add_(axis, idx, src)
def _segment_sum(src, seg, n):
    out = torch.zeros((n,) + src.shape[1:], dtype=src.dtype)
    return out.index_add_(0, seg, src)
def _pad(x, pw):
    flat = tuple(v for pair in reversed(pw) for v in pair)
    return torch.nn.functional.pad(x, flat)

kernel_name = 'gated_mix__softmax_full_mha__ssm_conv_h3__prompt'

def setup_inputs():

    _w = (lambda a: (lambda t: t.to(torch.bfloat16) if t.is_floating_point() else t)(torch.as_tensor(a)))
    def _A_setup():
        B, S, D = 1, 2048, 640
        return {
            "x": _w((lambda *s: torch.randn(*s, dtype=torch.bfloat16) * 0.05)(B, S, D)),
            "wqkv": _w((lambda *s: torch.randn(*s, dtype=torch.bfloat16) * 0.8)(D, 3 * D)),
            "wo": _w((lambda *s: torch.randn(*s, dtype=torch.bfloat16) * 0.05)(D, D)),
        }
    _A_in = _A_setup()

    def _B_setup():
        B, S, D = 1, 2048, 640
        ks = 4
        return {
            "q": _w((lambda *s: torch.randn(*s, dtype=torch.bfloat16) * 0.05)(B, S, D)),
            "k": _w((lambda *s: torch.randn(*s, dtype=torch.bfloat16) * 0.8)(B, S, D)),
            "v": _w((lambda *s: torch.randn(*s, dtype=torch.bfloat16) * 0.4)(B, S, D)),
            "shift_k": _w((lambda *s: torch.randn(*s, dtype=torch.bfloat16) * 0.8)(D, ks) * 0.5),
            "ssm_k": _w((lambda *s: torch.randn(*s, dtype=torch.bfloat16) * 0.8)(D, S) * (1.0 / S ** 0.5)),
            "ssm_D": _w((lambda *s: torch.randn(*s, dtype=torch.bfloat16) * 0.4)(D)),
        }
    _B_in = _B_setup()

    return {
        "A_x": _A_in["x"],
        "A_wqkv": _A_in["wqkv"],
        "A_wo": _A_in["wo"],
        "B_q": _B_in["q"],
        "B_k": _B_in["k"],
        "B_v": _B_in["v"],
        "B_shift_k": _B_in["shift_k"],
        "B_ssm_k": _B_in["ssm_k"],
        "B_ssm_D": _B_in["ssm_D"],
        "gate_w": _w((lambda *s: torch.randn(*s, dtype=torch.bfloat16) * 0.05)(640) * 16.0),
    }

def reference(A_x, A_wqkv, A_wo, B_q, B_k, B_v, B_shift_k, B_ssm_k, B_ssm_D, gate_w):
    def _A_ref(x, wqkv, wo):
        B, S, D = x.shape
        H = 32; Dh = D // H
        qkv = (lambda x, *d: x.permute(*d))((x @ wqkv).reshape(B, S, 3, H, Dh), 2, 0, 3, 1, 4)
        q, k, v = qkv[0], qkv[1], qkv[2]
        scale = Dh ** -0.5
        scores = torch.einsum("bhqd,bhkd->bhqk", q, k) * scale
        p = _softmax(scores, axis=-1)
        o = torch.einsum("bhqk,bhkd->bhqd", p, v)
        return (lambda x, *d: x.permute(*d))(o, 0, 2, 1, 3).reshape(B, S, D) @ wo

    def _B_ref(q, k, v, shift_k, ssm_k, ssm_D):
        B, S, D = q.shape
        ks = shift_k.shape[-1]
        k_pad = _pad(k, ((0, 0), (ks - 1, 0), (0, 0)))
        k_shift = torch.zeros_like(k)
        for t in range(ks):
            k_shift = k_shift + k_pad[:, t:t + S, :] * shift_k[:, ks - 1 - t][None, None, :]
        kv = k_shift * v
        L = 2 * S
        kv_pad = _pad(kv, ((0, 0), (0, S), (0, 0)))
        kf_pad = _pad(ssm_k, ((0, 0), (0, S)))
        kv_f = (lambda x, axes=(-2,-1): torch.fft.rfft2(x.float(), dim=axes))(kv_pad[:, None, :, :], axes=(1, 2))
        kf_f = (lambda x, axes=(-2,-1): torch.fft.rfft2(x.float(), dim=axes))(kf_pad[:, None, :], axes=(1, 2))
        kf_f = (lambda x, *d: x.permute(*d))(kf_f, 1, 2, 0)
        y_f = kv_f * kf_f[None, :, :, :]
        y = (lambda x, s, axes=(-2,-1): torch.fft.irfft2(x, s=s, dim=axes).to(torch.bfloat16))(y_f, (1, L), axes=(1, 2))[:, 0, :S, :]
        y = y + kv * ssm_D[None, None, :]
        return q * y

    x = A_x
    g = _sigmoid((x * gate_w).sum(-1, keepdims=True))
    a = _A_ref(x, A_wqkv, A_wo)
    b = _B_ref(x, B_k, B_v, B_shift_k, B_ssm_k, B_ssm_D)
    return g * a + (1 - g) * b
```

```cpp
#define FORCE_SLOW 0
#include <hip/hip_runtime.h>
#include <hip/hip_bf16.h>
#include <stdint.h>

typedef unsigned short u16;
typedef __attribute__((ext_vector_type(8))) short bf16x8;
typedef __attribute__((ext_vector_type(4))) short s16x4;
typedef __attribute__((ext_vector_type(16))) float f32x16;
typedef __attribute__((ext_vector_type(4))) float f32x4;
typedef __attribute__((ext_vector_type(4))) unsigned u32x4;
typedef __attribute__((ext_vector_type(2))) unsigned u32x2;
typedef __attribute__((address_space(3))) const char* lds_cptr;
typedef __attribute__((address_space(3))) char* lds_ptr;

#define MFMA32(a, b, c) __builtin_amdgcn_mfma_f32_32x32x16_bf16(a, b, c, 0, 0, 0)
#define MFMA32H(a, b, c) __builtin_amdgcn_mfma_f32_32x32x8bf16_1k(a, b, c, 0, 0, 0)
#define MFMA16(a, b, c) __builtin_amdgcn_mfma_f32_16x16x32_bf16(a, b, c, 0, 0, 0)
#define RFL(x) __builtin_amdgcn_readfirstlane(x)
#define DEV __device__ __forceinline__

constexpr int TILE = 3840;
constexpr size_t OFF_QKV = 0;
constexpr size_t OFF_U = 8u << 20;
constexpr size_t OFF_O = 11u << 20;
constexpr size_t OFF_YY = 14u << 20;
constexpr size_t OFF_G = 17u << 20;
constexpr float QSCALE = 0.22360679774997896f * 1.4426950408889634f;

typedef __attribute__((ext_vector_type(2))) float f32x2;
typedef __attribute__((ext_vector_type(2))) __bf16 bf16x2;
DEV unsigned cvtpk(float lo, float hi) { return __builtin_bit_cast(unsigned, __builtin_convertvector((f32x2){lo, hi}, bf16x2)); }
DEV float bflo(unsigned u) { return __uint_as_float(u << 16); }
DEV float bfhi(unsigned u) { return __uint_as_float(u & 0xffff0000u); }
DEV void glds16(const void* sbase, unsigned voff, unsigned lds_base) {
    unsigned sv;
    asm volatile("s_mov_b32 %0, m0\n\ts_mov_b32 m0, %2\n\ts_nop 0\n\tglobal_load_lds_dwordx4 %1, %3\n\ts_mov_b32 m0, %0"
                 : "=&s"(sv) : "v"(voff), "s"(lds_base), "s"(sbase) : "memory");
}
DEV s16x4 ldtr(lds_cptr p) { return __builtin_bit_cast(s16x4, __builtin_amdgcn_ds_read_tr16_b64_v4i16((__attribute__((address_space(3))) s16x4*)p)); }
DEV bf16x8 ldtr2(lds_cptr p0, lds_cptr p1) { s16x4 a = ldtr(p0), b = ldtr(p1); return (bf16x8){a[0], a[1], a[2], a[3], b[0], b[1], b[2], b[3]}; }
DEV bf16x8 ld128(lds_cptr p) { return *(const __attribute__((address_space(3))) bf16x8*)p; }
template <int N> DEV void wait_vm_bar() { asm volatile("s_waitcnt vmcnt(%0)\n\ts_barrier" ::"i"(N) : "memory"); }
DEV void wait_lgkm_bar() { asm volatile("s_waitcnt lgkmcnt(0)\n\ts_barrier" ::: "memory"); }

constexpr int K1_XS = 16384, K1_SLOT = 31744;

template <int CH>
DEV void k1_mfma(lds_cptr lds, int l, int th, int i, int p, char* ws) {
    const int c16 = l & 15, G = l >> 4, q = c16 >> 2, pp = l & 3;
    const int xo0 = (64 * th + c16) * 128 + ((G ^ (c16 >> 1)) * 16);
    const int xo1 = (64 * th + c16) * 128 + (((4 + G) ^ (c16 >> 1)) * 16);
    const int wo = K1_XS + (16 * (G >> 1) + 8 * (G & 1) + 2 * q) * 240 + CH * 128 + pp * 8;
    f32x4 acc[4][4];
#pragma unroll
    for (int a = 0; a < 4; ++a)
#pragma unroll
        for (int b = 0; b < 4; ++b) acc[a][b] = (f32x4){0.f, 0.f, 0.f, 0.f};
    bf16x8 xf[2][4], wf[2][4];
    auto ldfr = [&](int st, bf16x8* xs, bf16x8* wsf) {
        const int c = st >> 1, ks = st & 1;
        lds_cptr sl = lds + (c % 5) * K1_SLOT;
#pragma unroll
        for (int m = 0; m < 4; ++m) {
            xs[m] = ld128(sl + (ks ? xo1 : xo0) + m * 2048);
            wsf[m] = ldtr2(sl + wo + ks * 7680 + m * 32, sl + wo + ks * 7680 + 240 + m * 32);
        }
    };
    wait_lgkm_bar();
    ldfr(0, xf[0], wf[0]);
#pragma unroll
    for (int st = 0; st < 20; ++st) {
        if (st + 1 < 20) {
            if (((st + 1) & 1) == 0) wait_lgkm_bar();
            ldfr(st + 1, xf[(st + 1) & 1], wf[(st + 1) & 1]);
        }
#pragma unroll
        for (int T = 0; T < 4; ++T)
#pragma unroll
            for (int m = 0; m < 4; ++m) {
                if (CH == 0 || T == 0) acc[m][T] = MFMA16(wf[st & 1][T], xf[st & 1][m], acc[m][T]);
                else acc[m][T] = MFMA16(xf[st & 1][m], wf[st & 1][T], acc[m][T]);
            }
    }
    char* tb = ws + OFF_QKV + (size_t)((2 * p) * 64 + 4 * i + 2 * th) * TILE;
#pragma unroll
    for (int T = 0; T < 4; ++T) {
        if (CH == 0 || T == 0) {
            const int gam = 4 * (4 * CH + T) + G, kind = gam / 5, g = gam - 5 * kind;
            const float sc = kind < 2 ? QSCALE : 1.0f;
            const int base = (kind & 1) * 64 * TILE + (kind < 2 ? 2560 : 0);
            const int o0 = g < 4 ? (c16 + 32 * (g >> 1)) * 16 + 8 * (g & 1) : 1024 + c16 * 8;
            const int st16 = g < 4 ? 256 : 128;
#pragma unroll
            for (int m = 0; m < 4; ++m) {
                u32x2 v = {cvtpk(acc[m][T][0] * sc, acc[m][T][1] * sc), cvtpk(acc[m][T][2] * sc, acc[m][T][3] * sc)};
                *(u32x2*)(tb + base + (m >> 1) * TILE + o0 + (m & 1) * st16) = v;
            }
        } else {
            const int vc = 16 * (T - 1) + c16, hd = vc >= 20 ? 1 : 0, d = vc - 20 * hd;
            const int base = hd * 64 * TILE + 1280 + ((G & 1) * 20 + d) * 16 + 8 * (G >> 1);
            if (vc < 40) {
#pragma unroll
                for (int m = 0; m < 4; ++m) {
                    u32x2 v = {cvtpk(acc[m][T][0], acc[m][T][1]), cvtpk(acc[m][T][2], acc[m][T][3])};
                    *(u32x2*)(tb + base + (m >> 1) * TILE + (m & 1) * 640) = v;
                }
            }
        }
    }
}

DEV u32x4 aload16(const void* sbase, unsigned voff) { u32x4 r; asm volatile("global_load_dwordx4 %0, %1, %2" : "=v"(r) : "v"(voff), "s"(sbase) : "memory"); return r; }

__global__ __launch_bounds__(512, 2) void k1_qkv(const u16* __restrict__ x, const u16* __restrict__ W, const u16* __restrict__ bk,
                                                 const u16* __restrict__ bv, const u16* __restrict__ shk, char* __restrict__ ws) {
    __shared__ __attribute__((aligned(16))) char lds[5 * K1_SLOT + 64];
    const int tid = threadIdx.x, l = tid & 63, w = RFL(tid >> 6);
    const int b = blockIdx.x, xc = b & 7, wi = b >> 3;
    const int i = 8 * (xc >> 2) + (wi & 7), p = 4 * (xc & 3) + (wi >> 3);
    if (w < 4) {
        if (w < 2) k1_mfma<0>((lds_cptr)lds, l, w & 1, i, p, ws);
        else k1_mfma<1>((lds_cptr)lds, l, w & 1, i, p, ws);
        return;
    }
    const int hw = w - 4;
    const unsigned lds0 = (unsigned)(uintptr_t)lds;
    unsigned xvo[4], wvo[4];
#pragma unroll
    for (int n = 0; n < 4; ++n) {
        const int row = 32 * hw + 8 * n + (l >> 3);
        xvo[n] = (128 * i + row) * 1280 + (((l & 7) ^ ((row >> 1) & 7)) * 16);
        const int np = min(4 * hw + n, 14), P = 64 * np + l, rho = P / 15, pc = P - 15 * rho, pi = rho & 31;
        const int k = (rho & 32) | (pi & 24) | ((pi & 1) << 2) | ((pi >> 1) & 3), sg = pc / 5;
        wvo[n] = k * 3840 + sg * 1280 + 80 * p + (pc - 5 * sg) * 16;
    }
    auto issue = [&](int c) {
        const unsigned sl = lds0 + (c % 5) * K1_SLOT;
#pragma unroll
        for (int n = 0; n < 4; ++n) {
            glds16((const char*)x + c * 128, xvo[n], sl + (32 * hw + 8 * n) * 128);
            glds16((const char*)W + c * 64 * 3840, wvo[n], sl + K1_XS + min(4 * hw + n, 14) * 1024);
        }
    };
    issue(0);
    const int l40 = l < 40 ? l : l - 40, tg = l40 / 5, co = l40 - 5 * tg;
    const int t0 = 128 * i + 32 * hw + 4 * tg, c0 = 40 * p + 8 * co;
    u32x4 kr[7], vr[4], sr[4];
#pragma unroll
    for (int r = 0; r < 7; ++r) kr[r] = aload16(bk, max(t0 - 3 + r, 0) * 1280 + c0 * 2);
#pragma unroll
    for (int r = 0; r < 4; ++r) vr[r] = aload16(bv, (t0 + r) * 1280 + c0 * 2);
#pragma unroll
    for (int r = 0; r < 4; ++r) sr[r] = aload16(shk, c0 * 8 + r * 16);
    issue(1); issue(2); issue(3); issue(4);
    wait_vm_bar<47>();
    asm volatile("s_waitcnt vmcnt(24)\n\ts_barrier"
                 : "+v"(kr[0]), "+v"(kr[1]), "+v"(kr[2]), "+v"(kr[3]), "+v"(kr[4]), "+v"(kr[5]), "+v"(kr[6]), "+v"(vr[0]), "+v"(vr[1]),
                   "+v"(vr[2]), "+v"(vr[3]), "+v"(sr[0]), "+v"(sr[1]), "+v"(sr[2]), "+v"(sr[3])::"memory");
    issue(5);
    if (t0 == 0) { kr[0] = (u32x4){0, 0, 0, 0}; kr[1] = kr[0]; kr[2] = kr[0]; }
    char* ub = ws + OFF_U + (size_t)c0 * 4096 + ((t0 >> 4) & 1) * 2048 + (t0 >> 5) * 32 + (t0 & 15) * 2;
    auto prep = [&](int cp) {
        float o[2][4];
#pragma unroll
        for (int hf = 0; hf < 2; ++hf) {
            const float s0 = bflo(sr[cp][2 * hf]), s1 = bfhi(sr[cp][2 * hf]), s2 = bflo(sr[cp][2 * hf + 1]), s3 = bfhi(sr[cp][2 * hf + 1]);
            float kk[7];
#pragma unroll
            for (int r = 0; r < 7; ++r) kk[r] = hf ? bfhi(kr[r][cp]) : bflo(kr[r][cp]);
#pragma unroll
            for (int r = 0; r < 4; ++r) {
                const float ks = kk[r + 3] * s0 + kk[r + 2] * s1 + kk[r + 1] * s2 + kk[r] * s3;
                o[hf][r] = ks * (hf ? bfhi(vr[r][cp]) : bflo(vr[r][cp]));
            }
        }
        u32x2 a = {cvtpk(o[0][0], o[0][1]), cvtpk(o[0][2], o[0][3])}, bb = {cvtpk(o[1][0], o[1][1]), cvtpk(o[1][2], o[1][3])};
        if (l < 40) {
            char* d0 = ub + (2 * cp) * 4096; char* d1 = d0 + 4096;
            asm volatile("global_store_dwordx2 %0, %1, off\n\tglobal_store_dwordx2 %2, %3, off" ::"v"(d0), "v"(a), "v"(d1), "v"(bb) : "memory");
        }
    };
    prep(0);
    wait_vm_bar<26>();
    issue(6);
    prep(1);
    wait_vm_bar<28>();
    issue(7);
    prep(2);
    wait_vm_bar<30>();
    issue(8);
    prep(3);
    wait_vm_bar<32>();
    issue(9);
    wait_vm_bar<30>();
    wait_vm_bar<20>();
    wait_vm_bar<10>();
    wait_vm_bar<0>();
}

constexpr int CV_H = 4224, CV_UH = 2304, CV_CH = CV_H + 2 * CV_UH;
constexpr int L2_RED = 0, L2_CV = 98304, L2_CARRY = L2_CV + 3 * CV_CH, L2_PART = L2_CARRY + 3 * 4 * 16, L2_MX = L2_PART + 3 * 4 * 32,
              L2_BYTES = L2_MX + 8 * 128 * 4;

DEV float dpp_shift(float cur, float prev) {
    int t = __builtin_amdgcn_update_dpp(0, __float_as_int(prev), 0x13C, 0xf, 0xf, false);
    t = __builtin_amdgcn_update_dpp(t, __float_as_int(cur), 0x138, 0xf, 0xf, false);
    return __int_as_float(t);
}

template <int PR>
DEV void conv_pair(lds_ptr lds, int l, int ci, int d, bf16x8 fD, char* ws) {
    constexpr int N1 = 2 * PR, N2 = 2 * PR + 1;
    const int gh = (l >> 4) & 1, q = (l & 15) >> 2, pp = l & 3, hh = l >> 5;
    lds_cptr Hb = lds + L2_CV + ci * CV_CH + 2 * (16 + 16 * gh + 4 * pp - 4 * q + 32 * hh);
    lds_cptr Ub = lds + L2_CV + ci * CV_CH + CV_H + (2 + 4 * gh + pp - hh) * 32 + 8 * q;
    f32x16 a1 = {}, a2 = {};
    bf16x8 Gv[4 * N2 + 4];
#pragma unroll
    for (int J = 0; J < 4 * N2 + 4; ++J) {
        const bf16x8 F = ldtr2(Hb + 128 * J + 32, Hb + 128 * J);
        if (J < 4) Gv[4 * N2 - J + 3] = ldtr2(Ub + 64 * (4 * N2 - J + 3), Ub + 64 * (4 * N2 - J + 3) + CV_UH);
        if (J < 4 * N1 + 4) {
            Gv[4 * N1 - J + 3] = ldtr2(Ub + 64 * (4 * N1 - J + 3), Ub + 64 * (4 * N1 - J + 3) + CV_UH);
            a1 = MFMA32(Gv[4 * N1 - J + 3], F, a1);
        }
        a2 = MFMA32(Gv[4 * N2 - J + 3], F, a2);
        if (J == 0) { a1 = MFMA32(Gv[4 * N1 + 3], fD, a1); a2 = MFMA32(Gv[4 * N2 + 3], fD, a2); }
    }
    float y[8], p3 = 0.f, p2 = 0.f, p1 = 0.f;
#pragma unroll
    for (int g = 0; g < 8; ++g) {
        const f32x16& A = g < 4 ? a1 : a2;
        const int r = 4 * (g & 3);
        const float z3 = A[r + 3];
        const float z2 = A[r + 2] + dpp_shift(z3, p3);
        const float z1 = A[r + 1] + dpp_shift(z2, p2);
        y[g] = A[r] + dpp_shift(z1, p1);
        p3 = z3; p2 = z2; p1 = z1;
    }
    typedef __attribute__((address_space(3))) float* lds_f;
    if (PR < 3 && l == 63) { lds_f c = (lds_f)(lds + L2_CARRY + (ci * 4 + PR + 1) * 16); c[0] = p1; c[1] = p2; c[2] = p3; }
    unsigned* yo = (unsigned*)(ws + OFF_YY + (size_t)d * 4096 + PR * 1024) + l;
    if (PR > 0 && l < 3) { lds_f c = (lds_f)(lds + L2_PART + (ci * 4 + PR) * 32 + l * 8); c[0] = y[0]; c[1] = y[1]; }
    else yo[0] = cvtpk(y[0], y[1]);
    yo[64] = cvtpk(y[2], y[3]);
    yo[128] = cvtpk(y[4], y[5]);
    yo[192] = cvtpk(y[6], y[7]);
}

DEV void attn_slow(lds_ptr lds, int l, int w, const char* qb, const char* kvb) {
    typedef __attribute__((address_space(3))) float* lds_f;
    const int r32 = l & 31, hi = l >> 5;
    lds_f mx = (lds_f)(lds + L2_MX);
    for (int n = 0; n < 4; ++n) {
        const bf16x8 q16 = *(const bf16x8*)(qb + n * TILE + l * 16);
        s16x4 q4 = *(const s16x4*)(qb + n * TILE + 1024 + r32 * 8);
        if (hi) q4 = (s16x4){0, 0, 0, 0};
        float m = -INFINITY;
        for (int kt = 0; kt < 16; ++kt) {
            const bf16x8 k16 = *(const bf16x8*)(kvb + kt * TILE + l * 16);
            const s16x4 k4 = *(const s16x4*)(kvb + kt * TILE + 1024 + r32 * 8);
            f32x16 s = MFMA32(k16, q16, (f32x16){});
            s = MFMA32H(k4, q4, s);
#pragma unroll
            for (int r = 0; r < 16; ++r) m = fmaxf(m, s[r]);
        }
        m = fmaxf(m, __shfl_xor(m, 32));
        if (!hi) mx[w * 128 + n * 32 + r32] = m;
    }
    __syncthreads();
    for (int n = 0; n < 4; ++n) {
        const bf16x8 q16 = *(const bf16x8*)(qb + n * TILE + l * 16);
        s16x4 q4 = *(const s16x4*)(qb + n * TILE + 1024 + r32 * 8);
        if (hi) q4 = (s16x4){0, 0, 0, 0};
        const int wb = (w & 4) * 128 + n * 32 + r32;
        const float m = fmaxf(fmaxf(mx[wb], mx[wb + 128]), fmaxf(mx[wb + 256], mx[wb + 384]));
        f32x16 o = {};
        for (int kt = 0; kt < 16; ++kt) {
            const bf16x8 k16 = *(const bf16x8*)(kvb + kt * TILE + l * 16);
            const s16x4 k4 = *(const s16x4*)(kvb + kt * TILE + 1024 + r32 * 8);
            bf16x8 v0, v1;
#pragma unroll
            for (int j = 0; j < 8; ++j) { v0[j] = 0x3F80; v1[j] = 0x3F80; }
            if (r32 < 20) {
                v0 = *(const bf16x8*)(kvb + kt * TILE + 1280 + (hi * 20 + r32) * 16);
                v1 = *(const bf16x8*)(kvb + kt * TILE + 1920 + (hi * 20 + r32) * 16);
            }
            f32x16 s = MFMA32(k16, q16, (f32x16){});
            s = MFMA32H(k4, q4, s);
            u32x4 pa, pb;
#pragma unroll
            for (int r = 0; r < 4; ++r) {
                pa[r] = cvtpk(__builtin_amdgcn_exp2f(s[2 * r] - m), __builtin_amdgcn_exp2f(s[2 * r + 1] - m));
                pb[r] = cvtpk(__builtin_amdgcn_exp2f(s[8 + 2 * r] - m), __builtin_amdgcn_exp2f(s[9 + 2 * r] - m));
            }
            o = MFMA32(v0, __builtin_bit_cast(bf16x8, pa), o);
            o = MFMA32(v1, __builtin_bit_cast(bf16x8, pb), o);
        }
#pragma unroll
        for (int rg = 0; rg < 3; ++rg)
            *(__attribute__((address_space(3))) f32x4*)(lds + L2_RED + ((w * 4 + n) * 3 + rg) * 1024 + l * 16) =
                (f32x4){o[4 * rg], o[4 * rg + 1], o[4 * rg + 2], o[4 * rg + 3]};
    }
}

DEV void attn_fast(unsigned ldsdst, int l, const char* qb, const char* kvb) {
    const uint64_t qa = (uint64_t)qb, ka = (uint64_t)kvb;
    const u32x4 srdK = {(unsigned)RFL((unsigned)ka), (unsigned)RFL((unsigned)(ka >> 32)), 16u * TILE, 0x20000u};
    const u32x4 srdQ = {(unsigned)RFL((unsigned)qa), (unsigned)RFL((unsigned)(qa >> 32)), 4u * TILE, 0x20000u};
    const unsigned vo16 = l * 16, vo4 = 1024 + (l & 31) * 8, voV = 1280 + ((l >> 5) * 20 + (l & 31)) * 16, la = ldsdst + l * 16;
    unsigned so, cnt;
    asm volatile(
        "v_mov_b32 v160, 0\n\t"
        "v_mov_b32 v161, 0\n\t"
        "v_mov_b32 v162, 0\n\t"
        "v_mov_b32 v163, 0\n\t"
        "v_mov_b32 v164, 0\n\t"
        "v_mov_b32 v165, 0\n\t"
        "v_mov_b32 v166, 0\n\t"
        "v_mov_b32 v167, 0\n\t"
        "v_mov_b32 v174, 0x3f803f80\n\t"
        "v_mov_b32 v175, 0x3f803f80\n\t"
        "v_mov_b32 v176, 0x3f803f80\n\t"
        "v_mov_b32 v177, 0x3f803f80\n\t"
        "v_mov_b32 v178, 0x3f803f80\n\t"
        "v_mov_b32 v179, 0x3f803f80\n\t"
        "v_mov_b32 v180, 0x3f803f80\n\t"
        "v_mov_b32 v181, 0x3f803f80\n\t"
        "v_mov_b32 v188, 0x3f803f80\n\t"
        "v_mov_b32 v189, 0x3f803f80\n\t"
        "v_mov_b32 v190, 0x3f803f80\n\t"
        "v_mov_b32 v191, 0x3f803f80\n\t"
        "v_mov_b32 v192, 0x3f803f80\n\t"
        "v_mov_b32 v193, 0x3f803f80\n\t"
        "v_mov_b32 v194, 0x3f803f80\n\t"
        "v_mov_b32 v195, 0x3f803f80\n\t"
        "v_mov_b32 v202, 0x3f803f80\n\t"
        "v_mov_b32 v203, 0x3f803f80\n\t"
        "v_mov_b32 v204, 0x3f803f80\n\t"
        "v_mov_b32 v205, 0x3f803f80\n\t"
        "v_mov_b32 v206, 0x3f803f80\n\t"
        "v_mov_b32 v207, 0x3f803f80\n\t"
        "v_mov_b32 v208, 0x3f803f80\n\t"
        "v_mov_b32 v209, 0x3f803f80\n\t"
        "v_mov_b32 v216, 0x3f803f80\n\t"
        "v_mov_b32 v217, 0x3f803f80\n\t"
        "v_mov_b32 v218, 0x3f803f80\n\t"
        "v_mov_b32 v219, 0x3f803f80\n\t"
        "v_mov_b32 v220, 0x3f803f80\n\t"
        "v_mov_b32 v221, 0x3f803f80\n\t"
        "v_mov_b32 v222, 0x3f803f80\n\t"
        "v_mov_b32 v223, 0x3f803f80\n\t"
        "s_nop 4\n\t"
        "s_mov_b32 %0, 0\n\t"
        "buffer_load_dwordx4 v[144:147], %2, %7, %0 offen\n\t"
        "s_mov_b64 exec, %9\n\t"
        "buffer_load_dwordx2 v[160:161], %3, %7, %0 offen\n\t"
        "s_mov_b64 exec, -1\n\t"
        "s_mov_b32 %0, 3840\n\t"
        "buffer_load_dwordx4 v[148:151], %2, %7, %0 offen\n\t"
        "s_mov_b64 exec, %9\n\t"
        "buffer_load_dwordx2 v[162:163], %3, %7, %0 offen\n\t"
        "s_mov_b64 exec, -1\n\t"
        "s_mov_b32 %0, 7680\n\t"
        "buffer_load_dwordx4 v[152:155], %2, %7, %0 offen\n\t"
        "s_mov_b64 exec, %9\n\t"
        "buffer_load_dwordx2 v[164:165], %3, %7, %0 offen\n\t"
        "s_mov_b64 exec, -1\n\t"
        "s_mov_b32 %0, 11520\n\t"
        "buffer_load_dwordx4 v[156:159], %2, %7, %0 offen\n\t"
        "s_mov_b64 exec, %9\n\t"
        "buffer_load_dwordx2 v[166:167], %3, %7, %0 offen\n\t"
        "s_mov_b64 exec, -1\n\t"
        "s_mov_b32 %0, 0\n\t"
        "buffer_load_dwordx4 v[168:171], %2, %6, %0 offen\n\t"
        "buffer_load_dwordx2 v[172:173], %3, %6, %0 offen\n\t"
        "s_mov_b64 exec, %8\n\t"
        "buffer_load_dwordx4 v[174:177], %4, %6, %0 offen\n\t"
        "buffer_load_dwordx4 v[178:181], %4, %6, %0 offen offset:640\n\t"
        "s_mov_b64 exec, -1\n\t"
        "s_mov_b32 %0, 3840\n\t"
        "buffer_load_dwordx4 v[182:185], %2, %6, %0 offen\n\t"
        "buffer_load_dwordx2 v[186:187], %3, %6, %0 offen\n\t"
        "s_mov_b64 exec, %8\n\t"
        "buffer_load_dwordx4 v[188:191], %4, %6, %0 offen\n\t"
        "buffer_load_dwordx4 v[192:195], %4, %6, %0 offen offset:640\n\t"
        "s_mov_b64 exec, -1\n\t"
        "s_mov_b32 %0, 7680\n\t"
        "buffer_load_dwordx4 v[196:199], %2, %6, %0 offen\n\t"
        "buffer_load_dwordx2 v[200:201], %3, %6, %0 offen\n\t"
        "s_mov_b64 exec, %8\n\t"
        "buffer_load_dwordx4 v[202:205], %4, %6, %0 offen\n\t"
        "buffer_load_dwordx4 v[206:209], %4, %6, %0 offen offset:640\n\t"
        "s_mov_b64 exec, -1\n\t"
        "s_mov_b32 %0, 11520\n\t"
        "s_mov_b32 %1, 4\n\t"
        "v_mov_b32 v32, 0\n\t"
        "v_mov_b32 v33, 0\n\t"
        "v_mov_b32 v34, 0\n\t"
        "v_mov_b32 v35, 0\n\t"
        "v_mov_b32 v36, 0\n\t"
        "v_mov_b32 v37, 0\n\t"
        "v_mov_b32 v38, 0\n\t"
        "v_mov_b32 v39, 0\n\t"
        "v_mov_b32 v40, 0\n\t"
        "v_mov_b32 v41, 0\n\t"
        "v_mov_b32 v42, 0\n\t"
        "v_mov_b32 v43, 0\n\t"
        "v_mov_b32 v44, 0\n\t"
        "v_mov_b32 v45, 0\n\t"
        "v_mov_b32 v46, 0\n\t"
        "v_mov_b32 v47, 0\n\t"
        "v_mov_b32 v48, 0\n\t"
        "v_mov_b32 v49, 0\n\t"
        "v_mov_b32 v50, 0\n\t"
        "v_mov_b32 v51, 0\n\t"
        "v_mov_b32 v52, 0\n\t"
        "v_mov_b32 v53, 0\n\t"
        "v_mov_b32 v54, 0\n\t"
        "v_mov_b32 v55, 0\n\t"
        "v_mov_b32 v56, 0\n\t"
        "v_mov_b32 v57, 0\n\t"
        "v_mov_b32 v58, 0\n\t"
        "v_mov_b32 v59, 0\n\t"
        "v_mov_b32 v60, 0\n\t"
        "v_mov_b32 v61, 0\n\t"
        "v_mov_b32 v62, 0\n\t"
        "v_mov_b32 v63, 0\n\t"
        "v_mov_b32 v64, 0\n\t"
        "v_mov_b32 v65, 0\n\t"
        "v_mov_b32 v66, 0\n\t"
        "v_mov_b32 v67, 0\n\t"
        "v_mov_b32 v68, 0\n\t"
        "v_mov_b32 v69, 0\n\t"
        "v_mov_b32 v70, 0\n\t"
        "v_mov_b32 v71, 0\n\t"
        "v_mov_b32 v72, 0\n\t"
        "v_mov_b32 v73, 0\n\t"
        "v_mov_b32 v74, 0\n\t"
        "v_mov_b32 v75, 0\n\t"
        "v_mov_b32 v76, 0\n\t"
        "v_mov_b32 v77, 0\n\t"
        "v_mov_b32 v78, 0\n\t"
        "v_mov_b32 v79, 0\n\t"
        "v_mov_b32 v80, 0\n\t"
        "v_mov_b32 v81, 0\n\t"
        "v_mov_b32 v82, 0\n\t"
        "v_mov_b32 v83, 0\n\t"
        "v_mov_b32 v84, 0\n\t"
        "v_mov_b32 v85, 0\n\t"
        "v_mov_b32 v86, 0\n\t"
        "v_mov_b32 v87, 0\n\t"
        "v_mov_b32 v88, 0\n\t"
        "v_mov_b32 v89, 0\n\t"
        "v_mov_b32 v90, 0\n\t"
        "v_mov_b32 v91, 0\n\t"
        "v_mov_b32 v92, 0\n\t"
        "v_mov_b32 v93, 0\n\t"
        "v_mov_b32 v94, 0\n\t"
        "v_mov_b32 v95, 0\n\t"
        "v_mov_b32 v136, 0\n\t"
        "v_mov_b32 v137, 0\n\t"
        "v_mov_b32 v138, 0\n\t"
        "v_mov_b32 v139, 0\n\t"
        "v_mov_b32 v140, 0\n\t"
        "v_mov_b32 v141, 0\n\t"
        "v_mov_b32 v142, 0\n\t"
        "v_mov_b32 v143, 0\n\t"
        "s_waitcnt vmcnt(8)\n\t"
        "v_mfma_f32_32x32x16_bf16 v[96:111], v[168:171], v[144:147], 0\n\t"
        "s_nop 1\n\t"
        "v_mfma_f32_32x32x8_bf16 v[96:111], v[172:173], v[160:161], v[96:111]\n\t"
        "s_nop 7\n\t"
        "s_nop 3\n\t"
        "LOOP%=:\n\t"
        "v_mfma_f32_32x32x16_bf16 v[112:127], v[168:171], v[148:151], 0\n\t"
        "buffer_load_dwordx4 v[210:213], %2, %6, %0 offen\n\t"
        "v_exp_f32 v96, v96\n\t"
        "v_exp_f32 v97, v97\n\t"
        "v_exp_f32 v98, v98\n\t"
        "v_exp_f32 v99, v99\n\t"
        "v_exp_f32 v100, v100\n\t"
        "v_mfma_f32_32x32x16_bf16 v[80:95], v[216:219], v[136:139], v[80:95]\n\t"
        "v_exp_f32 v101, v101\n\t"
        "v_exp_f32 v102, v102\n\t"
        "v_exp_f32 v103, v103\n\t"
        "v_exp_f32 v104, v104\n\t"
        "v_exp_f32 v105, v105\n\t"
        "v_mfma_f32_32x32x8_bf16 v[112:127], v[172:173], v[162:163], v[112:127]\n\t"
        "v_exp_f32 v106, v106\n\t"
        "v_exp_f32 v107, v107\n\t"
        "v_exp_f32 v108, v108\n\t"
        "v_exp_f32 v109, v109\n\t"
        "v_exp_f32 v110, v110\n\t"
        "v_exp_f32 v111, v111\n\t"
        "v_mfma_f32_32x32x16_bf16 v[80:95], v[220:223], v[140:143], v[80:95]\n\t"
        "v_cvt_pk_bf16_f32 v128, v96, v97\n\t"
        "v_cvt_pk_bf16_f32 v129, v98, v99\n\t"
        "v_cvt_pk_bf16_f32 v130, v100, v101\n\t"
        "v_cvt_pk_bf16_f32 v131, v102, v103\n\t"
        "v_cvt_pk_bf16_f32 v132, v104, v105\n\t"
        "v_cvt_pk_bf16_f32 v133, v106, v107\n\t"
        "v_cvt_pk_bf16_f32 v134, v108, v109\n\t"
        "v_cvt_pk_bf16_f32 v135, v110, v111\n\t"
        "v_mfma_f32_32x32x16_bf16 v[96:111], v[168:171], v[152:155], 0\n\t"
        "buffer_load_dwordx2 v[214:215], %3, %6, %0 offen\n\t"
        "v_exp_f32 v112, v112\n\t"
        "v_exp_f32 v113, v113\n\t"
        "v_exp_f32 v114, v114\n\t"
        "v_exp_f32 v115, v115\n\t"
        "v_exp_f32 v116, v116\n\t"
        "v_mfma_f32_32x32x16_bf16 v[32:47], v[174:177], v[128:131], v[32:47]\n\t"
        "v_exp_f32 v117, v117\n\t"
        "v_exp_f32 v118, v118\n\t"
        "v_exp_f32 v119, v119\n\t"
        "v_exp_f32 v120, v120\n\t"
        "v_exp_f32 v121, v121\n\t"
        "v_mfma_f32_32x32x8_bf16 v[96:111], v[172:173], v[164:165], v[96:111]\n\t"
        "v_exp_f32 v122, v122\n\t"
        "v_exp_f32 v123, v123\n\t"
        "v_exp_f32 v124, v124\n\t"
        "v_exp_f32 v125, v125\n\t"
        "v_exp_f32 v126, v126\n\t"
        "v_exp_f32 v127, v127\n\t"
        "v_mfma_f32_32x32x16_bf16 v[32:47], v[178:181], v[132:135], v[32:47]\n\t"
        "v_cvt_pk_bf16_f32 v136, v112, v113\n\t"
        "v_cvt_pk_bf16_f32 v137, v114, v115\n\t"
        "v_cvt_pk_bf16_f32 v138, v116, v117\n\t"
        "v_cvt_pk_bf16_f32 v139, v118, v119\n\t"
        "v_cvt_pk_bf16_f32 v140, v120, v121\n\t"
        "v_cvt_pk_bf16_f32 v141, v122, v123\n\t"
        "v_cvt_pk_bf16_f32 v142, v124, v125\n\t"
        "v_cvt_pk_bf16_f32 v143, v126, v127\n\t"
        "v_mfma_f32_32x32x16_bf16 v[112:127], v[168:171], v[156:159], 0\n\t"
        "s_mov_b64 exec, %8\n\t"
        "buffer_load_dwordx4 v[216:219], %4, %6, %0 offen\n\t"
        "buffer_load_dwordx4 v[220:223], %4, %6, %0 offen offset:640\n\t"
        "s_mov_b64 exec, -1\n\t"
        "v_exp_f32 v96, v96\n\t"
        "v_exp_f32 v97, v97\n\t"
        "v_exp_f32 v98, v98\n\t"
        "v_exp_f32 v99, v99\n\t"
        "v_exp_f32 v100, v100\n\t"
        "v_mfma_f32_32x32x16_bf16 v[48:63], v[174:177], v[136:139], v[48:63]\n\t"
        "v_exp_f32 v101, v101\n\t"
        "v_exp_f32 v102, v102\n\t"
        "v_exp_f32 v103, v103\n\t"
        "v_exp_f32 v104, v104\n\t"
        "v_exp_f32 v105, v105\n\t"
        "v_mfma_f32_32x32x8_bf16 v[112:127], v[172:173], v[166:167], v[112:127]\n\t"
        "v_exp_f32 v106, v106\n\t"
        "v_exp_f32 v107, v107\n\t"
        "v_exp_f32 v108, v108\n\t"
        "v_exp_f32 v109, v109\n\t"
        "v_exp_f32 v110, v110\n\t"
        "v_exp_f32 v111, v111\n\t"
        "v_mfma_f32_32x32x16_bf16 v[48:63], v[178:181], v[140:143], v[48:63]\n\t"
        "v_cvt_pk_bf16_f32 v128, v96, v97\n\t"
        "v_cvt_pk_bf16_f32 v129, v98, v99\n\t"
        "v_cvt_pk_bf16_f32 v130, v100, v101\n\t"
        "v_cvt_pk_bf16_f32 v131, v102, v103\n\t"
        "v_cvt_pk_bf16_f32 v132, v104, v105\n\t"
        "v_cvt_pk_bf16_f32 v133, v106, v107\n\t"
        "v_cvt_pk_bf16_f32 v134, v108, v109\n\t"
        "v_cvt_pk_bf16_f32 v135, v110, v111\n\t"
        "s_waitcnt vmcnt(8)\n\t"
        "v_mfma_f32_32x32x16_bf16 v[96:111], v[182:185], v[144:147], 0\n\t"
        "s_add_u32 %0, %0, 3840\n\t"
        "v_exp_f32 v112, v112\n\t"
        "v_exp_f32 v113, v113\n\t"
        "v_exp_f32 v114, v114\n\t"
        "v_exp_f32 v115, v115\n\t"
        "v_exp_f32 v116, v116\n\t"
        "v_mfma_f32_32x32x16_bf16 v[64:79], v[174:177], v[128:131], v[64:79]\n\t"
        "v_exp_f32 v117, v117\n\t"
        "v_exp_f32 v118, v118\n\t"
        "v_exp_f32 v119, v119\n\t"
        "v_exp_f32 v120, v120\n\t"
        "v_exp_f32 v121, v121\n\t"
        "v_mfma_f32_32x32x8_bf16 v[96:111], v[186:187], v[160:161], v[96:111]\n\t"
        "v_exp_f32 v122, v122\n\t"
        "v_exp_f32 v123, v123\n\t"
        "v_exp_f32 v124, v124\n\t"
        "v_exp_f32 v125, v125\n\t"
        "v_exp_f32 v126, v126\n\t"
        "v_exp_f32 v127, v127\n\t"
        "v_mfma_f32_32x32x16_bf16 v[64:79], v[178:181], v[132:135], v[64:79]\n\t"
        "v_cvt_pk_bf16_f32 v136, v112, v113\n\t"
        "v_cvt_pk_bf16_f32 v137, v114, v115\n\t"
        "v_cvt_pk_bf16_f32 v138, v116, v117\n\t"
        "v_cvt_pk_bf16_f32 v139, v118, v119\n\t"
        "v_cvt_pk_bf16_f32 v140, v120, v121\n\t"
        "v_cvt_pk_bf16_f32 v141, v122, v123\n\t"
        "v_cvt_pk_bf16_f32 v142, v124, v125\n\t"
        "v_cvt_pk_bf16_f32 v143, v126, v127\n\t"
        "v_mfma_f32_32x32x16_bf16 v[112:127], v[182:185], v[148:151], 0\n\t"
        "buffer_load_dwordx4 v[168:171], %2, %6, %0 offen\n\t"
        "v_exp_f32 v96, v96\n\t"
        "v_exp_f32 v97, v97\n\t"
        "v_exp_f32 v98, v98\n\t"
        "v_exp_f32 v99, v99\n\t"
        "v_exp_f32 v100, v100\n\t"
        "v_mfma_f32_32x32x16_bf16 v[80:95], v[174:177], v[136:139], v[80:95]\n\t"
        "v_exp_f32 v101, v101\n\t"
        "v_exp_f32 v102, v102\n\t"
        "v_exp_f32 v103, v103\n\t"
        "v_exp_f32 v104, v104\n\t"
        "v_exp_f32 v105, v105\n\t"
        "v_mfma_f32_32x32x8_bf16 v[112:127], v[186:187], v[162:163], v[112:127]\n\t"
        "v_exp_f32 v106, v106\n\t"
        "v_exp_f32 v107, v107\n\t"
        "v_exp_f32 v108, v108\n\t"
        "v_exp_f32 v109, v109\n\t"
        "v_exp_f32 v110, v110\n\t"
        "v_exp_f32 v111, v111\n\t"
        "v_mfma_f32_32x32x16_bf16 v[80:95], v[178:181], v[140:143], v[80:95]\n\t"
        "v_cvt_pk_bf16_f32 v128, v96, v97\n\t"
        "v_cvt_pk_bf16_f32 v129, v98, v99\n\t"
        "v_cvt_pk_bf16_f32 v130, v100, v101\n\t"
        "v_cvt_pk_bf16_f32 v131, v102, v103\n\t"
        "v_cvt_pk_bf16_f32 v132, v104, v105\n\t"
        "v_cvt_pk_bf16_f32 v133, v106, v107\n\t"
        "v_cvt_pk_bf16_f32 v134, v108, v109\n\t"
        "v_cvt_pk_bf16_f32 v135, v110, v111\n\t"
        "v_mfma_f32_32x32x16_bf16 v[96:111], v[182:185], v[152:155], 0\n\t"
        "buffer_load_dwordx2 v[172:173], %3, %6, %0 offen\n\t"
        "v_exp_f32 v112, v112\n\t"
        "v_exp_f32 v113, v113\n\t"
        "v_exp_f32 v114, v114\n\t"
        "v_exp_f32 v115, v115\n\t"
        "v_exp_f32 v116, v116\n\t"
        "v_mfma_f32_32x32x16_bf16 v[32:47], v[188:191], v[128:131], v[32:47]\n\t"
        "v_exp_f32 v117, v117\n\t"
        "v_exp_f32 v118, v118\n\t"
        "v_exp_f32 v119, v119\n\t"
        "v_exp_f32 v120, v120\n\t"
        "v_exp_f32 v121, v121\n\t"
        "v_mfma_f32_32x32x8_bf16 v[96:111], v[186:187], v[164:165], v[96:111]\n\t"
        "v_exp_f32 v122, v122\n\t"
        "v_exp_f32 v123, v123\n\t"
        "v_exp_f32 v124, v124\n\t"
        "v_exp_f32 v125, v125\n\t"
        "v_exp_f32 v126, v126\n\t"
        "v_exp_f32 v127, v127\n\t"
        "v_mfma_f32_32x32x16_bf16 v[32:47], v[192:195], v[132:135], v[32:47]\n\t"
        "v_cvt_pk_bf16_f32 v136, v112, v113\n\t"
        "v_cvt_pk_bf16_f32 v137, v114, v115\n\t"
        "v_cvt_pk_bf16_f32 v138, v116, v117\n\t"
        "v_cvt_pk_bf16_f32 v139, v118, v119\n\t"
        "v_cvt_pk_bf16_f32 v140, v120, v121\n\t"
        "v_cvt_pk_bf16_f32 v141, v122, v123\n\t"
        "v_cvt_pk_bf16_f32 v142, v124, v125\n\t"
        "v_cvt_pk_bf16_f32 v143, v126, v127\n\t"
        "v_mfma_f32_32x32x16_bf16 v[112:127], v[182:185], v[156:159], 0\n\t"
        "s_mov_b64 exec, %8\n\t"
        "buffer_load_dwordx4 v[174:177], %4, %6, %0 offen\n\t"
        "buffer_load_dwordx4 v[178:181], %4, %6, %0 offen offset:640\n\t"
        "s_mov_b64 exec, -1\n\t"
        "v_exp_f32 v96, v96\n\t"
        "v_exp_f32 v97, v97\n\t"
        "v_exp_f32 v98, v98\n\t"
        "v_exp_f32 v99, v99\n\t"
        "v_exp_f32 v100, v100\n\t"
        "v_mfma_f32_32x32x16_bf16 v[48:63], v[188:191], v[136:139], v[48:63]\n\t"
        "v_exp_f32 v101, v101\n\t"
        "v_exp_f32 v102, v102\n\t"
        "v_exp_f32 v103, v103\n\t"
        "v_exp_f32 v104, v104\n\t"
        "v_exp_f32 v105, v105\n\t"
        "v_mfma_f32_32x32x8_bf16 v[112:127], v[186:187], v[166:167], v[112:127]\n\t"
        "v_exp_f32 v106, v106\n\t"
        "v_exp_f32 v107, v107\n\t"
        "v_exp_f32 v108, v108\n\t"
        "v_exp_f32 v109, v109\n\t"
        "v_exp_f32 v110, v110\n\t"
        "v_exp_f32 v111, v111\n\t"
        "v_mfma_f32_32x32x16_bf16 v[48:63], v[192:195], v[140:143], v[48:63]\n\t"
        "v_cvt_pk_bf16_f32 v128, v96, v97\n\t"
        "v_cvt_pk_bf16_f32 v129, v98, v99\n\t"
        "v_cvt_pk_bf16_f32 v130, v100, v101\n\t"
        "v_cvt_pk_bf16_f32 v131, v102, v103\n\t"
        "v_cvt_pk_bf16_f32 v132, v104, v105\n\t"
        "v_cvt_pk_bf16_f32 v133, v106, v107\n\t"
        "v_cvt_pk_bf16_f32 v134, v108, v109\n\t"
        "v_cvt_pk_bf16_f32 v135, v110, v111\n\t"
        "s_waitcnt vmcnt(8)\n\t"
        "v_mfma_f32_32x32x16_bf16 v[96:111], v[196:199], v[144:147], 0\n\t"
        "s_add_u32 %0, %0, 3840\n\t"
        "v_exp_f32 v112, v112\n\t"
        "v_exp_f32 v113, v113\n\t"
        "v_exp_f32 v114, v114\n\t"
        "v_exp_f32 v115, v115\n\t"
        "v_exp_f32 v116, v116\n\t"
        "v_mfma_f32_32x32x16_bf16 v[64:79], v[188:191], v[128:131], v[64:79]\n\t"
        "v_exp_f32 v117, v117\n\t"
        "v_exp_f32 v118, v118\n\t"
        "v_exp_f32 v119, v119\n\t"
        "v_exp_f32 v120, v120\n\t"
        "v_exp_f32 v121, v121\n\t"
        "v_mfma_f32_32x32x8_bf16 v[96:111], v[200:201], v[160:161], v[96:111]\n\t"
        "v_exp_f32 v122, v122\n\t"
        "v_exp_f32 v123, v123\n\t"
        "v_exp_f32 v124, v124\n\t"
        "v_exp_f32 v125, v125\n\t"
        "v_exp_f32 v126, v126\n\t"
        "v_exp_f32 v127, v127\n\t"
        "v_mfma_f32_32x32x16_bf16 v[64:79], v[192:195], v[132:135], v[64:79]\n\t"
        "v_cvt_pk_bf16_f32 v136, v112, v113\n\t"
        "v_cvt_pk_bf16_f32 v137, v114, v115\n\t"
        "v_cvt_pk_bf16_f32 v138, v116, v117\n\t"
        "v_cvt_pk_bf16_f32 v139, v118, v119\n\t"
        "v_cvt_pk_bf16_f32 v140, v120, v121\n\t"
        "v_cvt_pk_bf16_f32 v141, v122, v123\n\t"
        "v_cvt_pk_bf16_f32 v142, v124, v125\n\t"
        "v_cvt_pk_bf16_f32 v143, v126, v127\n\t"
        "v_mfma_f32_32x32x16_bf16 v[112:127], v[196:199], v[148:151], 0\n\t"
        "buffer_load_dwordx4 v[182:185], %2, %6, %0 offen\n\t"
        "v_exp_f32 v96, v96\n\t"
        "v_exp_f32 v97, v97\n\t"
        "v_exp_f32 v98, v98\n\t"
        "v_exp_f32 v99, v99\n\t"
        "v_exp_f32 v100, v100\n\t"
        "v_mfma_f32_32x32x16_bf16 v[80:95], v[188:191], v[136:139], v[80:95]\n\t"
        "v_exp_f32 v101, v101\n\t"
        "v_exp_f32 v102, v102\n\t"
        "v_exp_f32 v103, v103\n\t"
        "v_exp_f32 v104, v104\n\t"
        "v_exp_f32 v105, v105\n\t"
        "v_mfma_f32_32x32x8_bf16 v[112:127], v[200:201], v[162:163], v[112:127]\n\t"
        "v_exp_f32 v106, v106\n\t"
        "v_exp_f32 v107, v107\n\t"
        "v_exp_f32 v108, v108\n\t"
        "v_exp_f32 v109, v109\n\t"
        "v_exp_f32 v110, v110\n\t"
        "v_exp_f32 v111, v111\n\t"
        "v_mfma_f32_32x32x16_bf16 v[80:95], v[192:195], v[140:143], v[80:95]\n\t"
        "v_cvt_pk_bf16_f32 v128, v96, v97\n\t"
        "v_cvt_pk_bf16_f32 v129, v98, v99\n\t"
        "v_cvt_pk_bf16_f32 v130, v100, v101\n\t"
        "v_cvt_pk_bf16_f32 v131, v102, v103\n\t"
        "v_cvt_pk_bf16_f32 v132, v104, v105\n\t"
        "v_cvt_pk_bf16_f32 v133, v106, v107\n\t"
        "v_cvt_pk_bf16_f32 v134, v108, v109\n\t"
        "v_cvt_pk_bf16_f32 v135, v110, v111\n\t"
        "v_mfma_f32_32x32x16_bf16 v[96:111], v[196:199], v[152:155], 0\n\t"
        "buffer_load_dwordx2 v[186:187], %3, %6, %0 offen\n\t"
        "v_exp_f32 v112, v112\n\t"
        "v_exp_f32 v113, v113\n\t"
        "v_exp_f32 v114, v114\n\t"
        "v_exp_f32 v115, v115\n\t"
        "v_exp_f32 v116, v116\n\t"
        "v_mfma_f32_32x32x16_bf16 v[32:47], v[202:205], v[128:131], v[32:47]\n\t"
        "v_exp_f32 v117, v117\n\t"
        "v_exp_f32 v118, v118\n\t"
        "v_exp_f32 v119, v119\n\t"
        "v_exp_f32 v120, v120\n\t"
        "v_exp_f32 v121, v121\n\t"
        "v_mfma_f32_32x32x8_bf16 v[96:111], v[200:201], v[164:165], v[96:111]\n\t"
        "v_exp_f32 v122, v122\n\t"
        "v_exp_f32 v123, v123\n\t"
        "v_exp_f32 v124, v124\n\t"
        "v_exp_f32 v125, v125\n\t"
        "v_exp_f32 v126, v126\n\t"
        "v_exp_f32 v127, v127\n\t"
        "v_mfma_f32_32x32x16_bf16 v[32:47], v[206:209], v[132:135], v[32:47]\n\t"
        "v_cvt_pk_bf16_f32 v136, v112, v113\n\t"
        "v_cvt_pk_bf16_f32 v137, v114, v115\n\t"
        "v_cvt_pk_bf16_f32 v138, v116, v117\n\t"
        "v_cvt_pk_bf16_f32 v139, v118, v119\n\t"
        "v_cvt_pk_bf16_f32 v140, v120, v121\n\t"
        "v_cvt_pk_bf16_f32 v141, v122, v123\n\t"
        "v_cvt_pk_bf16_f32 v142, v124, v125\n\t"
        "v_cvt_pk_bf16_f32 v143, v126, v127\n\t"
        "v_mfma_f32_32x32x16_bf16 v[112:127], v[196:199], v[156:159], 0\n\t"
        "s_mov_b64 exec, %8\n\t"
        "buffer_load_dwordx4 v[188:191], %4, %6, %0 offen\n\t"
        "buffer_load_dwordx4 v[192:195], %4, %6, %0 offen offset:640\n\t"
        "s_mov_b64 exec, -1\n\t"
        "v_exp_f32 v96, v96\n\t"
        "v_exp_f32 v97, v97\n\t"
        "v_exp_f32 v98, v98\n\t"
        "v_exp_f32 v99, v99\n\t"
        "v_exp_f32 v100, v100\n\t"
        "v_mfma_f32_32x32x16_bf16 v[48:63], v[202:205], v[136:139], v[48:63]\n\t"
        "v_exp_f32 v101, v101\n\t"
        "v_exp_f32 v102, v102\n\t"
        "v_exp_f32 v103, v103\n\t"
        "v_exp_f32 v104, v104\n\t"
        "v_exp_f32 v105, v105\n\t"
        "v_mfma_f32_32x32x8_bf16 v[112:127], v[200:201], v[166:167], v[112:127]\n\t"
        "v_exp_f32 v106, v106\n\t"
        "v_exp_f32 v107, v107\n\t"
        "v_exp_f32 v108, v108\n\t"
        "v_exp_f32 v109, v109\n\t"
        "v_exp_f32 v110, v110\n\t"
        "v_exp_f32 v111, v111\n\t"
        "v_mfma_f32_32x32x16_bf16 v[48:63], v[206:209], v[140:143], v[48:63]\n\t"
        "v_cvt_pk_bf16_f32 v128, v96, v97\n\t"
        "v_cvt_pk_bf16_f32 v129, v98, v99\n\t"
        "v_cvt_pk_bf16_f32 v130, v100, v101\n\t"
        "v_cvt_pk_bf16_f32 v131, v102, v103\n\t"
        "v_cvt_pk_bf16_f32 v132, v104, v105\n\t"
        "v_cvt_pk_bf16_f32 v133, v106, v107\n\t"
        "v_cvt_pk_bf16_f32 v134, v108, v109\n\t"
        "v_cvt_pk_bf16_f32 v135, v110, v111\n\t"
        "s_waitcnt vmcnt(8)\n\t"
        "v_mfma_f32_32x32x16_bf16 v[96:111], v[210:213], v[144:147], 0\n\t"
        "s_add_u32 %0, %0, 3840\n\t"
        "v_exp_f32 v112, v112\n\t"
        "v_exp_f32 v113, v113\n\t"
        "v_exp_f32 v114, v114\n\t"
        "v_exp_f32 v115, v115\n\t"
        "v_exp_f32 v116, v116\n\t"
        "v_mfma_f32_32x32x16_bf16 v[64:79], v[202:205], v[128:131], v[64:79]\n\t"
        "v_exp_f32 v117, v117\n\t"
        "v_exp_f32 v118, v118\n\t"
        "v_exp_f32 v119, v119\n\t"
        "v_exp_f32 v120, v120\n\t"
        "v_exp_f32 v121, v121\n\t"
        "v_mfma_f32_32x32x8_bf16 v[96:111], v[214:215], v[160:161], v[96:111]\n\t"
        "v_exp_f32 v122, v122\n\t"
        "v_exp_f32 v123, v123\n\t"
        "v_exp_f32 v124, v124\n\t"
        "v_exp_f32 v125, v125\n\t"
        "v_exp_f32 v126, v126\n\t"
        "v_exp_f32 v127, v127\n\t"
        "v_mfma_f32_32x32x16_bf16 v[64:79], v[206:209], v[132:135], v[64:79]\n\t"
        "v_cvt_pk_bf16_f32 v136, v112, v113\n\t"
        "v_cvt_pk_bf16_f32 v137, v114, v115\n\t"
        "v_cvt_pk_bf16_f32 v138, v116, v117\n\t"
        "v_cvt_pk_bf16_f32 v139, v118, v119\n\t"
        "v_cvt_pk_bf16_f32 v140, v120, v121\n\t"
        "v_cvt_pk_bf16_f32 v141, v122, v123\n\t"
        "v_cvt_pk_bf16_f32 v142, v124, v125\n\t"
        "v_cvt_pk_bf16_f32 v143, v126, v127\n\t"
        "v_mfma_f32_32x32x16_bf16 v[112:127], v[210:213], v[148:151], 0\n\t"
        "buffer_load_dwordx4 v[196:199], %2, %6, %0 offen\n\t"
        "v_exp_f32 v96, v96\n\t"
        "v_exp_f32 v97, v97\n\t"
        "v_exp_f32 v98, v98\n\t"
        "v_exp_f32 v99, v99\n\t"
        "v_exp_f32 v100, v100\n\t"
        "v_mfma_f32_32x32x16_bf16 v[80:95], v[202:205], v[136:139], v[80:95]\n\t"
        "v_exp_f32 v101, v101\n\t"
        "v_exp_f32 v102, v102\n\t"
        "v_exp_f32 v103, v103\n\t"
        "v_exp_f32 v104, v104\n\t"
        "v_exp_f32 v105, v105\n\t"
        "v_mfma_f32_32x32x8_bf16 v[112:127], v[214:215], v[162:163], v[112:127]\n\t"
        "v_exp_f32 v106, v106\n\t"
        "v_exp_f32 v107, v107\n\t"
        "v_exp_f32 v108, v108\n\t"
        "v_exp_f32 v109, v109\n\t"
        "v_exp_f32 v110, v110\n\t"
        "v_exp_f32 v111, v111\n\t"
        "v_mfma_f32_32x32x16_bf16 v[80:95], v[206:209], v[140:143], v[80:95]\n\t"
        "v_cvt_pk_bf16_f32 v128, v96, v97\n\t"
        "v_cvt_pk_bf16_f32 v129, v98, v99\n\t"
        "v_cvt_pk_bf16_f32 v130, v100, v101\n\t"
        "v_cvt_pk_bf16_f32 v131, v102, v103\n\t"
        "v_cvt_pk_bf16_f32 v132, v104, v105\n\t"
        "v_cvt_pk_bf16_f32 v133, v106, v107\n\t"
        "v_cvt_pk_bf16_f32 v134, v108, v109\n\t"
        "v_cvt_pk_bf16_f32 v135, v110, v111\n\t"
        "v_mfma_f32_32x32x16_bf16 v[96:111], v[210:213], v[152:155], 0\n\t"
        "buffer_load_dwordx2 v[200:201], %3, %6, %0 offen\n\t"
        "v_exp_f32 v112, v112\n\t"
        "v_exp_f32 v113, v113\n\t"
        "v_exp_f32 v114, v114\n\t"
        "v_exp_f32 v115, v115\n\t"
        "v_exp_f32 v116, v116\n\t"
        "v_mfma_f32_32x32x16_bf16 v[32:47], v[216:219], v[128:131], v[32:47]\n\t"
        "v_exp_f32 v117, v117\n\t"
        "v_exp_f32 v118, v118\n\t"
        "v_exp_f32 v119, v119\n\t"
        "v_exp_f32 v120, v120\n\t"
        "v_exp_f32 v121, v121\n\t"
        "v_mfma_f32_32x32x8_bf16 v[96:111], v[214:215], v[164:165], v[96:111]\n\t"
        "v_exp_f32 v122, v122\n\t"
        "v_exp_f32 v123, v123\n\t"
        "v_exp_f32 v124, v124\n\t"
        "v_exp_f32 v125, v125\n\t"
        "v_exp_f32 v126, v126\n\t"
        "v_exp_f32 v127, v127\n\t"
        "v_mfma_f32_32x32x16_bf16 v[32:47], v[220:223], v[132:135], v[32:47]\n\t"
        "v_cvt_pk_bf16_f32 v136, v112, v113\n\t"
        "v_cvt_pk_bf16_f32 v137, v114, v115\n\t"
        "v_cvt_pk_bf16_f32 v138, v116, v117\n\t"
        "v_cvt_pk_bf16_f32 v139, v118, v119\n\t"
        "v_cvt_pk_bf16_f32 v140, v120, v121\n\t"
        "v_cvt_pk_bf16_f32 v141, v122, v123\n\t"
        "v_cvt_pk_bf16_f32 v142, v124, v125\n\t"
        "v_cvt_pk_bf16_f32 v143, v126, v127\n\t"
        "v_mfma_f32_32x32x16_bf16 v[112:127], v[210:213], v[156:159], 0\n\t"
        "s_mov_b64 exec, %8\n\t"
        "buffer_load_dwordx4 v[202:205], %4, %6, %0 offen\n\t"
        "buffer_load_dwordx4 v[206:209], %4, %6, %0 offen offset:640\n\t"
        "s_mov_b64 exec, -1\n\t"
        "v_exp_f32 v96, v96\n\t"
        "v_exp_f32 v97, v97\n\t"
        "v_exp_f32 v98, v98\n\t"
        "v_exp_f32 v99, v99\n\t"
        "v_exp_f32 v100, v100\n\t"
        "v_mfma_f32_32x32x16_bf16 v[48:63], v[216:219], v[136:139], v[48:63]\n\t"
        "v_exp_f32 v101, v101\n\t"
        "v_exp_f32 v102, v102\n\t"
        "v_exp_f32 v103, v103\n\t"
        "v_exp_f32 v104, v104\n\t"
        "v_exp_f32 v105, v105\n\t"
        "v_mfma_f32_32x32x8_bf16 v[112:127], v[214:215], v[166:167], v[112:127]\n\t"
        "v_exp_f32 v106, v106\n\t"
        "v_exp_f32 v107, v107\n\t"
        "v_exp_f32 v108, v108\n\t"
        "v_exp_f32 v109, v109\n\t"
        "v_exp_f32 v110, v110\n\t"
        "v_exp_f32 v111, v111\n\t"
        "v_mfma_f32_32x32x16_bf16 v[48:63], v[220:223], v[140:143], v[48:63]\n\t"
        "v_cvt_pk_bf16_f32 v128, v96, v97\n\t"
        "v_cvt_pk_bf16_f32 v129, v98, v99\n\t"
        "v_cvt_pk_bf16_f32 v130, v100, v101\n\t"
        "v_cvt_pk_bf16_f32 v131, v102, v103\n\t"
        "v_cvt_pk_bf16_f32 v132, v104, v105\n\t"
        "v_cvt_pk_bf16_f32 v133, v106, v107\n\t"
        "v_cvt_pk_bf16_f32 v134, v108, v109\n\t"
        "v_cvt_pk_bf16_f32 v135, v110, v111\n\t"
        "s_waitcnt vmcnt(8)\n\t"
        "v_mfma_f32_32x32x16_bf16 v[96:111], v[168:171], v[144:147], 0\n\t"
        "s_add_u32 %0, %0, 3840\n\t"
        "s_sub_u32 %1, %1, 1\n\t"
        "s_cmp_lg_u32 %1, 0\n\t"
        "v_exp_f32 v112, v112\n\t"
        "v_exp_f32 v113, v113\n\t"
        "v_exp_f32 v114, v114\n\t"
        "v_exp_f32 v115, v115\n\t"
        "v_exp_f32 v116, v116\n\t"
        "v_mfma_f32_32x32x16_bf16 v[64:79], v[216:219], v[128:131], v[64:79]\n\t"
        "v_exp_f32 v117, v117\n\t"
        "v_exp_f32 v118, v118\n\t"
        "v_exp_f32 v119, v119\n\t"
        "v_exp_f32 v120, v120\n\t"
        "v_exp_f32 v121, v121\n\t"
        "v_mfma_f32_32x32x8_bf16 v[96:111], v[172:173], v[160:161], v[96:111]\n\t"
        "v_exp_f32 v122, v122\n\t"
        "v_exp_f32 v123, v123\n\t"
        "v_exp_f32 v124, v124\n\t"
        "v_exp_f32 v125, v125\n\t"
        "v_exp_f32 v126, v126\n\t"
        "v_exp_f32 v127, v127\n\t"
        "v_mfma_f32_32x32x16_bf16 v[64:79], v[220:223], v[132:135], v[64:79]\n\t"
        "v_cvt_pk_bf16_f32 v136, v112, v113\n\t"
        "v_cvt_pk_bf16_f32 v137, v114, v115\n\t"
        "v_cvt_pk_bf16_f32 v138, v116, v117\n\t"
        "v_cvt_pk_bf16_f32 v139, v118, v119\n\t"
        "v_cvt_pk_bf16_f32 v140, v120, v121\n\t"
        "v_cvt_pk_bf16_f32 v141, v122, v123\n\t"
        "v_cvt_pk_bf16_f32 v142, v124, v125\n\t"
        "v_cvt_pk_bf16_f32 v143, v126, v127\n\t"
        "s_cbranch_scc1 LOOP%=\n\t"
        "s_nop 1\n\t"
        "v_mfma_f32_32x32x16_bf16 v[80:95], v[216:219], v[136:139], v[80:95]\n\t"
        "v_mfma_f32_32x32x16_bf16 v[80:95], v[220:223], v[140:143], v[80:95]\n\t"
        "ds_write_b128 %5, v[32:35] offset:0\n\t"
        "ds_write_b128 %5, v[36:39] offset:1024\n\t"
        "ds_write_b128 %5, v[40:43] offset:2048\n\t"
        "ds_write_b128 %5, v[48:51] offset:3072\n\t"
        "ds_write_b128 %5, v[52:55] offset:4096\n\t"
        "ds_write_b128 %5, v[56:59] offset:5120\n\t"
        "ds_write_b128 %5, v[64:67] offset:6144\n\t"
        "ds_write_b128 %5, v[68:71] offset:7168\n\t"
        "ds_write_b128 %5, v[72:75] offset:8192\n\t"
        "s_nop 7\n\t"
        "ds_write_b128 %5, v[80:83] offset:9216\n\t"
        "ds_write_b128 %5, v[84:87] offset:10240\n\t"
        "ds_write_b128 %5, v[88:91] offset:11264\n\t"
        "s_waitcnt vmcnt(0) lgkmcnt(0)\n\t"
        : "=&s"(so), "=&s"(cnt)
        : "v"(vo16), "v"(vo4), "v"(voV), "v"(la), "s"(srdK), "s"(srdQ), "s"(0x000FFFFF000FFFFFull), "s"(0x00000000FFFFFFFFull)
        : "memory", "scc", "v32", "v33", "v34", "v35", "v36", "v37", "v38", "v39", "v40", "v41", "v42", "v43", "v44", "v45", "v46", "v47", "v48", "v49", "v50", "v51", "v52", "v53", "v54", "v55", "v56", "v57", "v58", "v59", "v60", "v61", "v62", "v63", "v64", "v65", "v66", "v67", "v68", "v69", "v70", "v71", "v72", "v73", "v74", "v75", "v76", "v77", "v78", "v79", "v80", "v81", "v82", "v83", "v84", "v85", "v86", "v87", "v88", "v89", "v90", "v91", "v92", "v93", "v94", "v95", "v96", "v97", "v98", "v99", "v100", "v101", "v102", "v103", "v104", "v105", "v106", "v107", "v108", "v109", "v110", "v111", "v112", "v113", "v114", "v115", "v116", "v117", "v118", "v119", "v120", "v121", "v122", "v123", "v124", "v125", "v126", "v127", "v128", "v129", "v130", "v131", "v132", "v133", "v134", "v135", "v136", "v137", "v138", "v139", "v140", "v141", "v142", "v143", "v144", "v145", "v146", "v147", "v148", "v149", "v150", "v151", "v152", "v153", "v154", "v155", "v156", "v157", "v158", "v159", "v160", "v161", "v162", "v163", "v164", "v165", "v166", "v167", "v168", "v169", "v170", "v171", "v172", "v173", "v174", "v175", "v176", "v177", "v178", "v179", "v180", "v181", "v182", "v183", "v184", "v185", "v186", "v187", "v188", "v189", "v190", "v191", "v192", "v193", "v194", "v195", "v196", "v197", "v198", "v199", "v200", "v201", "v202", "v203", "v204", "v205", "v206", "v207", "v208", "v209", "v210", "v211", "v212", "v213", "v214", "v215", "v216", "v217", "v218", "v219", "v220", "v221", "v222", "v223");
}

DEV bool attn_finish(lds_ptr lds, int l, int w, int i, int p, char* ws) {
    const int hd = w >> 2, n = w & 3, hi = l >> 5;
    f32x4 o[3];
#pragma unroll
    for (int rg = 0; rg < 3; ++rg) {
        o[rg] = (f32x4){0.f, 0.f, 0.f, 0.f};
#pragma unroll
        for (int kq = 0; kq < 4; ++kq) o[rg] += *(const __attribute__((address_space(3))) f32x4*)(lds + L2_RED + (((hd * 4 + kq) * 4 + n) * 3 + rg) * 1024 + l * 16);
    }
    const float lsum = __shfl(o[2][0], (l & 31) + 32);
    const float rl = 1.0f / lsum;
    float chk = 0.f;
#pragma unroll
    for (int rg = 0; rg < 3; ++rg)
#pragma unroll
        for (int e = 0; e < 4; ++e) chk += fabsf(o[rg][e]);
    const bool bad = !(lsum > 9.765625e-4f && lsum < 1.0e12f && chk < 1.0e30f);
    char* ob = ws + OFF_O + (size_t)(128 * i + 32 * n + (l & 31)) * 1280 + (2 * p + hd) * 40 + hi * 8;
#pragma unroll
    for (int rg = 0; rg < 3; ++rg)
        if (rg < 2 || !hi) *(u32x2*)(ob + rg * 16) = (u32x2){cvtpk(o[rg][0] * rl, o[rg][1] * rl), cvtpk(o[rg][2] * rl, o[rg][3] * rl)};
    return bad;
}

__global__ __launch_bounds__(512, 2) void k2_attn(const u16* __restrict__ x, const u16* __restrict__ gw, const u16* __restrict__ ssmk,
                                                  const u16* __restrict__ ssmD, char* __restrict__ ws) {
    __shared__ __attribute__((aligned(16))) char lds_[L2_BYTES];
    lds_ptr lds = (lds_ptr)lds_;
    const int tid = threadIdx.x, l = tid & 63, w = RFL(tid >> 6);
    const int b = blockIdx.x, xc = b & 7, wi = b >> 3;
    const int i = 8 * (xc >> 2) + (wi & 7), p = 4 * (xc & 3) + (wi >> 3);
    const unsigned lds0 = (unsigned)(uintptr_t)lds_;
    const int nch = b < 128 ? 3 : 2;
    for (int ci = 0; ci < nch; ++ci) {
        const int d = b + 256 * ci;
        if (w < 4) glds16((const char*)ssmk + (size_t)d * 4096 + w * 1024, l * 16, lds0 + L2_CV + ci * CV_CH + 64 + w * 1024);
        else glds16(ws + OFF_U + (size_t)d * 4096 + (w - 4) * 1024, l * 16, lds0 + L2_CV + ci * CV_CH + CV_H + ((w - 4) >> 1) * CV_UH + 256 + (w & 1) * 1024);
    }
    if (tid < 3 * 36) {
        const int ci = tid / 36, e = tid - 36 * ci;
        const int off = e < 4 ? e * 16 : (e < 20 ? CV_H + (e - 4) * 16 : CV_H + CV_UH + (e - 20) * 16);
        *(__attribute__((address_space(3))) u32x4*)(lds + L2_CV + ci * CV_CH + off) = (u32x4){0, 0, 0, 0};
    }
    {
        const int t = 128 * i + 8 * p + w;
        const unsigned* xr = (const unsigned*)(x + (size_t)t * 640) + l;
        const unsigned* gr = (const unsigned*)gw + l;
        float s = 0.f;
#pragma unroll
        for (int j = 0; j < 5; ++j) { const unsigned a = xr[64 * j], c = gr[64 * j]; s += bflo(a) * bflo(c) + bfhi(a) * bfhi(c); }
#pragma unroll
        for (int o = 32; o; o >>= 1) s += __shfl_xor(s, o);
        if (l == 0) ((float*)(ws + OFF_G))[t] = 1.0f / (1.0f + __expf(-s));
    }
    asm volatile("s_waitcnt vmcnt(0) lgkmcnt(0)\n\ts_barrier" ::: "memory");
    {
        unsigned lst;
        if (nch == 3) lst = w == 0 ? 0xF3 : w == 1 ? 0xF7 : w == 2 ? 0xFB : w == 3 ? 0xF0A : w == 4 ? 0xF2 : w == 5 ? 0xF6 : w == 6 ? 0xF51 : 0xF849;
        else lst = w == 0 ? 0xF3 : w == 1 ? 0xF7 : w == 2 ? 0xF2 : w == 3 ? 0xF6 : w == 4 ? 0xF0 : w == 5 ? 0xF4 : w == 6 ? 0xF1 : 0xF5;
        for (; (lst & 15) != 15; lst >>= 4) {
            const int ci = (lst & 15) >> 2, pr = lst & 3, d = b + 256 * ci;
            const short Dv = (short)ssmD[d];
            bf16x8 fD;
#pragma unroll
            for (int e = 0; e < 8; ++e) fD[e] = (l == 4 * e) ? Dv : (short)0;
            if (pr == 3) conv_pair<3>(lds, l, ci, d, fD, ws);
            else if (pr == 2) conv_pair<2>(lds, l, ci, d, fD, ws);
            else if (pr == 1) conv_pair<1>(lds, l, ci, d, fD, ws);
            else conv_pair<0>(lds, l, ci, d, fD, ws);
        }
    }
    const int hd = 2 * p + (w >> 2);
    const char* qb = ws + OFF_QKV + (size_t)(hd * 64 + 4 * i) * TILE + 2560;
    const char* kvb = ws + OFF_QKV + (size_t)(hd * 64 + 16 * (w & 3)) * TILE;
    attn_fast(lds0 + L2_RED + w * 12288, l, qb, kvb);
    __syncthreads();
    if (tid < nch * 9) {
        const int ci = tid / 9, r = tid - 9 * ci, pr = 1 + r / 3, e = r % 3;
        typedef __attribute__((address_space(3))) float* lds_f;
        const lds_f pt = (lds_f)(lds + L2_PART + (ci * 4 + pr) * 32 + e * 8);
        const float c = ((lds_f)(lds + L2_CARRY + (ci * 4 + pr) * 16))[e];
        ((unsigned*)(ws + OFF_YY + (size_t)(b + 256 * ci) * 4096 + pr * 1024))[e] = cvtpk(pt[0] + c, pt[1]);
    }
    const bool bad = attn_finish(lds, l, w, i, p, ws);
    if (__syncthreads_or(bad)) {
        attn_slow(lds, l, w, qb, kvb);
        __syncthreads();
        attn_finish(lds, l, w, i, p, ws);
    }
}

constexpr int K3_OS = 16384, K3_SLOT = 36864, K3_YY = 4 * K3_SLOT;

__global__ __launch_bounds__(512, 2) void k3_out(const u16* __restrict__ x, const u16* __restrict__ wo, const char* __restrict__ ws, u16* __restrict__ out) {
    __shared__ __attribute__((aligned(16))) char lds_[K3_YY + 10240];
    lds_cptr lds = (lds_cptr)lds_;
    const int tid = threadIdx.x, l = tid & 63, w = RFL(tid >> 6);
    const int b = blockIdx.x, xc = b & 7, wi = b >> 3;
    const int tt = 8 * (xc >> 1) + (wi & 7), nb = 4 * (xc & 1) + (wi >> 3);
    const int tbase = 128 * (tt >> 1) + 32 * (tt & 1);
    const unsigned lds0 = (unsigned)(uintptr_t)lds_;
    if (w >= 4) {
        const int hw = w - 4;
        unsigned ovo[4], wvo[5];
#pragma unroll
        for (int n = 0; n < 4; ++n) {
            const int c = 4 * n + (l >> 4);
            const int t = tbase + 64 * (c & 1) + 8 * hw + (c >> 1);
            ovo[n] = t * 1280 + (((l & 15) ^ c) * 16);
        }
#pragma unroll
        for (int n = 0; n < 5; ++n) {
            const int P = 64 * (5 * hw + n) + l, rho = P / 10, pc = P - 10 * rho;
            const int k = (rho & ~12) | ((rho & 4) << 1) | ((rho & 8) >> 1);
            wvo[n] = k * 1280 + 160 * nb + pc * 16;
        }
        auto issue = [&](int c) {
            const unsigned sl = lds0 + (c & 3) * K3_SLOT;
#pragma unroll
            for (int n = 0; n < 4; ++n) glds16(ws + OFF_O + c * 256, ovo[n], sl + (16 * hw + 4 * n) * 256);
#pragma unroll
            for (int n = 0; n < 5; ++n) glds16((const char*)wo + c * 128 * 1280, wvo[n], sl + K3_OS + (5 * hw + n) * 1024);
        };
        issue(0); issue(1); issue(2); issue(3);
#pragma unroll
        for (int n = 0; n < 3; ++n) {
            const int np = min(3 * hw + n, 9);
            glds16(ws + OFF_YY, (80 * nb + 8 * np + (l >> 3)) * 4096 + tt * 128 + (l & 7) * 16, lds0 + K3_YY + np * 1024);
        }
        wait_vm_bar<30>();
        wait_vm_bar<21>();
        issue(4);
        wait_vm_bar<21>();
        wait_vm_bar<12>();
        wait_vm_bar<0>();
        return;
    }
    const int c16 = l & 15, G = l >> 4, q = c16 >> 2, pp = l & 3;
    const int t = tbase + 64 * (c16 & 1) + 8 * w + (c16 >> 1);
    u32x2 xv[5];
#pragma unroll
    for (int nt = 0; nt < 5; ++nt) xv[nt] = *(const u32x2*)(x + (size_t)t * 640 + 80 * nb + 16 * nt + 4 * G);
    const float g = ((const float*)(ws + OFF_G))[t];
    const int oo = (16 * w + c16) * 256;
    const int wo_ = K3_OS + (16 * (G >> 1) + 4 * (G & 1) + q) * 160 + pp * 8;
    f32x4 acc[5];
#pragma unroll
    for (int nt = 0; nt < 5; ++nt) acc[nt] = (f32x4){0.f, 0.f, 0.f, 0.f};
    bf16x8 of[2], wf[2][5];
    auto ldfr = [&](int st, bf16x8& o, bf16x8* wv) {
        const int c = st >> 2, ks = st & 3;
        lds_cptr sl = lds + (c & 3) * K3_SLOT;
        o = ld128(sl + oo + (((4 * ks + G) ^ c16) * 16));
#pragma unroll
        for (int nt = 0; nt < 5; ++nt) wv[nt] = ldtr2(sl + wo_ + ks * 5120 + nt * 32, sl + wo_ + ks * 5120 + 1280 + nt * 32);
    };
    wait_lgkm_bar();
    ldfr(0, of[0], wf[0]);
#pragma unroll
    for (int st = 0; st < 20; ++st) {
        if (st + 1 < 20) {
            if (((st + 1) & 3) == 0) wait_lgkm_bar();
            ldfr(st + 1, of[(st + 1) & 1], wf[(st + 1) & 1]);
        }
#pragma unroll
        for (int nt = 0; nt < 5; ++nt) acc[nt] = MFMA16(wf[st & 1][nt], of[st & 1], acc[nt]);
    }
    const float g1 = 1.0f - g;
#pragma unroll
    for (int nt = 0; nt < 5; ++nt) {
        const u32x2 yv = __builtin_bit_cast(u32x2, ldtr(lds + K3_YY + (16 * nt + 4 * G + q) * 128 + 32 * w + 8 * pp));
        const float r0 = g * acc[nt][0] + g1 * bflo(xv[nt][0]) * bflo(yv[0]);
        const float r1 = g * acc[nt][1] + g1 * bfhi(xv[nt][0]) * bfhi(yv[0]);
        const float r2 = g * acc[nt][2] + g1 * bflo(xv[nt][1]) * bflo(yv[1]);
        const float r3 = g * acc[nt][3] + g1 * bfhi(xv[nt][1]) * bfhi(yv[1]);
        *(u32x2*)(out + (size_t)t * 640 + 80 * nb + 16 * nt + 4 * G) = (u32x2){cvtpk(r0, r1), cvtpk(r2, r3)};
    }
}

extern "C" void kernel_launch(void* const* d_in, const int* in_sizes, int n_in, void* d_out, int out_size, void* d_ws, size_t ws_size,
                              hipStream_t stream) {
    const u16* x = (const u16*)d_in[0];
    char* ws = (char*)d_ws;
    k1_qkv<<<256, 512, 0, stream>>>(x, (const u16*)d_in[1], (const u16*)d_in[4], (const u16*)d_in[5], (const u16*)d_in[6], ws);
    k2_attn<<<256, 512, 0, stream>>>(x, (const u16*)d_in[9], (const u16*)d_in[7], (const u16*)d_in[8], ws);
    k3_out<<<256, 512, 0, stream>>>(x, (const u16*)d_in[2], ws, (u16*)d_out);
}
```

```cpp
#define FORCE_SLOW 0
#define TSTAMP 0
#include <hip/hip_runtime.h>
#include <hip/hip_bf16.h>
#include <stdint.h>

typedef unsigned short u16;
typedef __attribute__((ext_vector_type(8))) short bf16x8;
typedef __attribute__((ext_vector_type(4))) short s16x4;
typedef __attribute__((ext_vector_type(16))) float f32x16;
typedef __attribute__((ext_vector_type(4))) float f32x4;
typedef __attribute__((ext_vector_type(4))) unsigned u32x4;
typedef __attribute__((ext_vector_type(2))) unsigned u32x2;
typedef __attribute__((address_space(3))) const char* lds_cptr;
typedef __attribute__((address_space(3))) char* lds_ptr;

#define MFMA32(a, b, c) __builtin_amdgcn_mfma_f32_32x32x16_bf16(a, b, c, 0, 0, 0)
#define MFMA32H(a, b, c) __builtin_amdgcn_mfma_f32_32x32x8bf16_1k(a, b, c, 0, 0, 0)
#define MFMA16(a, b, c) __builtin_amdgcn_mfma_f32_16x16x32_bf16(a, b, c, 0, 0, 0)
#define RFL(x) __builtin_amdgcn_readfirstlane(x)
#define DEV __device__ __forceinline__

#define TS(kid, slot)
constexpr int TILE = 3840;
constexpr size_t OFF_QKV = 0;
constexpr size_t OFF_U = 8u << 20;
constexpr size_t OFF_O = 11u << 20;
constexpr size_t OFF_YY = 14u << 20;
constexpr size_t OFF_G = 17u << 20;
constexpr float QSCALE = 0.22360679774997896f * 1.4426950408889634f;

typedef __attribute__((ext_vector_type(2))) float f32x2;
typedef __attribute__((ext_vector_type(2))) __bf16 bf16x2;
DEV unsigned cvtpk(float lo, float hi) { return __builtin_bit_cast(unsigned, __builtin_convertvector((f32x2){lo, hi}, bf16x2)); }
DEV float bflo(unsigned u) { return __uint_as_float(u << 16); }
DEV float bfhi(unsigned u) { return __uint_as_float(u & 0xffff0000u); }
DEV void glds16(const void* sbase, unsigned voff, unsigned lds_base) {
    unsigned sv;
    asm volatile("s_mov_b32 %0, m0\n\ts_mov_b32 m0, %2\n\ts_nop 0\n\tglobal_load_lds_dwordx4 %1, %3\n\ts_mov_b32 m0, %0"
                 : "=&s"(sv) : "v"(voff), "s"(lds_base), "s"(sbase) : "memory");
}
DEV s16x4 ldtr(lds_cptr p) { return __builtin_bit_cast(s16x4, __builtin_amdgcn_ds_read_tr16_b64_v4i16((__attribute__((address_space(3))) s16x4*)p)); }
DEV bf16x8 ldtr2(lds_cptr p0, lds_cptr p1) { s16x4 a = ldtr(p0), b = ldtr(p1); return (bf16x8){a[0], a[1], a[2], a[3], b[0], b[1], b[2], b[3]}; }
DEV bf16x8 ld128(lds_cptr p) { return *(const __attribute__((address_space(3))) bf16x8*)p; }
template <int N> DEV void wait_vm_bar() { asm volatile("s_waitcnt vmcnt(%0)\n\ts_barrier" ::"i"(N) : "memory"); }
DEV void wait_lgkm_bar() { asm volatile("s_waitcnt lgkmcnt(0)\n\ts_barrier" ::: "memory"); }

constexpr int K1_XS = 16384, K1_SLOT = 31744;

template <int CH>
DEV void k1_mfma(lds_cptr lds, int l, int th, int i, int p, char* ws) {
    const int c16 = l & 15, G = l >> 4, q = c16 >> 2, pp = l & 3;
    const int xo0 = (64 * th + c16) * 128 + ((G ^ (c16 >> 1)) * 16);
    const int xo1 = (64 * th + c16) * 128 + (((4 + G) ^ (c16 >> 1)) * 16);
    const int wo = K1_XS + (16 * (G >> 1) + 8 * (G & 1) + 2 * q) * 240 + CH * 128 + pp * 8;
    f32x4 acc[4][4];
#pragma unroll
    for (int a = 0; a < 4; ++a)
#pragma unroll
        for (int b = 0; b < 4; ++b) acc[a][b] = (f32x4){0.f, 0.f, 0.f, 0.f};
    bf16x8 xf[2][4], wf[2][4];
    auto ldfr = [&](int st, bf16x8* xs, bf16x8* wsf) {
        const int c = st >> 1, ks = st & 1;
        lds_cptr sl = lds + (c % 5) * K1_SLOT;
#pragma unroll
        for (int m = 0; m < 4; ++m) {
            xs[m] = ld128(sl + (ks ? xo1 : xo0) + m * 2048);
            wsf[m] = ldtr2(sl + wo + ks * 7680 + m * 32, sl + wo + ks * 7680 + 240 + m * 32);
        }
    };
    TS(0, 0);
    wait_lgkm_bar();
    TS(0, 1);
    ldfr(0, xf[0], wf[0]);
#pragma unroll
    for (int st = 0; st < 20; ++st) {
        if (st + 1 < 20) {
            if (((st + 1) & 1) == 0) wait_lgkm_bar();
            ldfr(st + 1, xf[(st + 1) & 1], wf[(st + 1) & 1]);
        }
#pragma unroll
        for (int T = 0; T < 4; ++T)
#pragma unroll
            for (int m = 0; m < 4; ++m) {
                if (CH == 0 || T == 0) acc[m][T] = MFMA16(wf[st & 1][T], xf[st & 1][m], acc[m][T]);
                else acc[m][T] = MFMA16(xf[st & 1][m], wf[st & 1][T], acc[m][T]);
            }
    }
    TS(0, 2);
    char* tb = ws + OFF_QKV + (size_t)((2 * p) * 64 + 4 * i + 2 * th) * TILE;
#pragma unroll
    for (int T = 0; T < 4; ++T) {
        if (CH == 0 || T == 0) {
            const int gam = 4 * (4 * CH + T) + G, kind = gam / 5, g = gam - 5 * kind;
            const float sc = kind < 2 ? QSCALE : 1.0f;
            const int base = (kind & 1) * 64 * TILE + (kind < 2 ? 2560 : 0);
            const int o0 = g < 4 ? (c16 + 32 * (g >> 1)) * 16 + 8 * (g & 1) : 1024 + c16 * 8;
            const int st16 = g < 4 ? 256 : 128;
#pragma unroll
            for (int m = 0; m < 4; ++m) {
                u32x2 v = {cvtpk(acc[m][T][0] * sc, acc[m][T][1] * sc), cvtpk(acc[m][T][2] * sc, acc[m][T][3] * sc)};
                *(u32x2*)(tb + base + (m >> 1) * TILE + o0 + (m & 1) * st16) = v;
            }
        } else {
            const int vc = 16 * (T - 1) + c16, hd = vc >= 20 ? 1 : 0, d = vc - 20 * hd;
            const int base = hd * 64 * TILE + 1280 + ((G & 1) * 20 + d) * 16 + 8 * (G >> 1);
            if (vc < 40) {
#pragma unroll
                for (int m = 0; m < 4; ++m) {
                    u32x2 v = {cvtpk(acc[m][T][0], acc[m][T][1]), cvtpk(acc[m][T][2], acc[m][T][3])};
                    *(u32x2*)(tb + base + (m >> 1) * TILE + (m & 1) * 640) = v;
                }
            }
        }
    }
}

DEV u32x4 aload16(const void* sbase, unsigned voff) { u32x4 r; asm volatile("global_load_dwordx4 %0, %1, %2" : "=v"(r) : "v"(voff), "s"(sbase) : "memory"); return r; }

__global__ __launch_bounds__(512, 2) void k1_qkv(const u16* __restrict__ x, const u16* __restrict__ W, const u16* __restrict__ bk,
                                                 const u16* __restrict__ bv, const u16* __restrict__ shk, char* __restrict__ ws) {
    __shared__ __attribute__((aligned(16))) char lds[5 * K1_SLOT + 64];
    const int tid = threadIdx.x, l = tid & 63, w = RFL(tid >> 6);
    const int b = blockIdx.x, xc = b & 7, wi = b >> 3;
    const int i = 8 * (xc >> 2) + (wi & 7), p = 4 * (xc & 3) + (wi >> 3);
    if (w < 4) {
        if (w < 2) k1_mfma<0>((lds_cptr)lds, l, w & 1, i, p, ws);
        else k1_mfma<1>((lds_cptr)lds, l, w & 1, i, p, ws);
        TS(0, 3);
        return;
    }
    const int hw = w - 4;
    const unsigned lds0 = (unsigned)(uintptr_t)lds;
    unsigned xvo[4], wvo[4];
#pragma unroll
    for (int n = 0; n < 4; ++n) {
        const int row = 32 * hw + 8 * n + (l >> 3);
        xvo[n] = (128 * i + row) * 1280 + (((l & 7) ^ ((row >> 1) & 7)) * 16);
        const int np = min(4 * hw + n, 14), P = 64 * np + l, rho = P / 15, pc = P - 15 * rho, pi = rho & 31;
        const int k = (rho & 32) | (pi & 24) | ((pi & 1) << 2) | ((pi >> 1) & 3), sg = pc / 5;
        wvo[n] = k * 3840 + sg * 1280 + 80 * p + (pc - 5 * sg) * 16;
    }
    auto issue = [&](int c) {
        const unsigned sl = lds0 + (c % 5) * K1_SLOT;
#pragma unroll
        for (int n = 0; n < 4; ++n) {
            glds16((const char*)x + c * 128, xvo[n], sl + (32 * hw + 8 * n) * 128);
            glds16((const char*)W + c * 64 * 3840, wvo[n], sl + K1_XS + min(4 * hw + n, 14) * 1024);
        }
    };
    issue(0);
    const int l40 = l < 40 ? l : l - 40, tg = l40 / 5, co = l40 - 5 * tg;
    const int t0 = 128 * i + 32 * hw + 4 * tg, c0 = 40 * p + 8 * co;
    u32x4 kr[7], vr[4], sr[4];
#pragma unroll
    for (int r = 0; r < 7; ++r) kr[r] = aload16(bk, max(t0 - 3 + r, 0) * 1280 + c0 * 2);
#pragma unroll
    for (int r = 0; r < 4; ++r) vr[r] = aload16(bv, (t0 + r) * 1280 + c0 * 2);
#pragma unroll
    for (int r = 0; r < 4; ++r) sr[r] = aload16(shk, c0 * 8 + r * 16);
    issue(1); issue(2); issue(3); issue(4);
    wait_vm_bar<47>();
    asm volatile("s_waitcnt vmcnt(24)\n\ts_barrier"
                 : "+v"(kr[0]), "+v"(kr[1]), "+v"(kr[2]), "+v"(kr[3]), "+v"(kr[4]), "+v"(kr[5]), "+v"(kr[6]), "+v"(vr[0]), "+v"(vr[1]),
                   "+v"(vr[2]), "+v"(vr[3]), "+v"(sr[0]), "+v"(sr[1]), "+v"(sr[2]), "+v"(sr[3])::"memory");
    issue(5);
    if (t0 == 0) { kr[0] = (u32x4){0, 0, 0, 0}; kr[1] = kr[0]; kr[2] = kr[0]; }
    char* ub = ws + OFF_U + (size_t)c0 * 4096 + ((t0 >> 4) & 1) * 2048 + (t0 >> 5) * 32 + (t0 & 15) * 2;
    auto prep = [&](int cp) {
        float o[2][4];
#pragma unroll
        for (int hf = 0; hf < 2; ++hf) {
            const float s0 = bflo(sr[cp][2 * hf]), s1 = bfhi(sr[cp][2 * hf]), s2 = bflo(sr[cp][2 * hf + 1]), s3 = bfhi(sr[cp][2 * hf + 1]);
            float kk[7];
#pragma unroll
            for (int r = 0; r < 7; ++r) kk[r] = hf ? bfhi(kr[r][cp]) : bflo(kr[r][cp]);
#pragma unroll
            for (int r = 0; r < 4; ++r) {
                const float ks = kk[r + 3] * s0 + kk[r + 2] * s1 + kk[r + 1] * s2 + kk[r] * s3;
                o[hf][r] = ks * (hf ? bfhi(vr[r][cp]) : bflo(vr[r][cp]));
            }
        }
        u32x2 a = {cvtpk(o[0][0], o[0][1]), cvtpk(o[0][2], o[0][3])}, bb = {cvtpk(o[1][0], o[1][1]), cvtpk(o[1][2], o[1][3])};
        if (l < 40) {
            char* d0 = ub + (2 * cp) * 4096; char* d1 = d0 + 4096;
            asm volatile("global_store_dwordx2 %0, %1, off\n\tglobal_store_dwordx2 %2, %3, off" ::"v"(d0), "v"(a), "v"(d1), "v"(bb) : "memory");
        }
    };
    prep(0);
    wait_vm_bar<26>();
    issue(6);
    prep(1);
    wait_vm_bar<28>();
    issue(7);
    prep(2);
    wait_vm_bar<30>();
    issue(8);
    prep(3);
    wait_vm_bar<32>();
    issue(9);
    wait_vm_bar<30>();
    wait_vm_bar<20>();
    wait_vm_bar<10>();
    wait_vm_bar<0>();
}

constexpr int CV_H = 4224, CV_UH = 2304, CV_CH = CV_H + 2 * CV_UH;
constexpr int L2_RED = 0, L2_CV = 98304, L2_CARRY = L2_CV + 3 * CV_CH, L2_PART = L2_CARRY + 3 * 4 * 16, L2_MX = L2_PART + 3 * 4 * 32,
              L2_BYTES = L2_MX + 8 * 128 * 4;

DEV float dpp_shift(float cur, float prev) {
    int t = __builtin_amdgcn_update_dpp(0, __float_as_int(prev), 0x13C, 0xf, 0xf, false);
    t = __builtin_amdgcn_update_dpp(t, __float_as_int(cur), 0x138, 0xf, 0xf, false);
    return __int_as_float(t);
}

template <int PR>
DEV void conv_pair(lds_ptr lds, int l, int ci, int d, bf16x8 fD, char* ws) {
    constexpr int N1 = 2 * PR, N2 = 2 * PR + 1;
    const int gh = (l >> 4) & 1, q = (l & 15) >> 2, pp = l & 3, hh = l >> 5;
    lds_cptr Hb = lds + L2_CV + ci * CV_CH + 2 * (16 + 16 * gh + 4 * pp - 4 * q + 32 * hh);
    lds_cptr Ub = lds + L2_CV + ci * CV_CH + CV_H + (2 + 4 * gh + pp - hh) * 32 + 8 * q;
    f32x16 a1 = {}, a2 = {};
    bf16x8 Gv[4 * N2 + 4];
#pragma unroll
    for (int J = 0; J < 4 * N2 + 4; ++J) {
        const bf16x8 F = ldtr2(Hb + 128 * J + 32, Hb + 128 * J);
        if (J < 4) Gv[4 * N2 - J + 3] = ldtr2(Ub + 64 * (4 * N2 - J + 3), Ub + 64 * (4 * N2 - J + 3) + CV_UH);
        if (J < 4 * N1 + 4) {
            Gv[4 * N1 - J + 3] = ldtr2(Ub + 64 * (4 * N1 - J + 3), Ub + 64 * (4 * N1 - J + 3) + CV_UH);
            a1 = MFMA32(Gv[4 * N1 - J + 3], F, a1);
        }
        a2 = MFMA32(Gv[4 * N2 - J + 3], F, a2);
        if (J == 0) { a1 = MFMA32(Gv[4 * N1 + 3], fD, a1); a2 = MFMA32(Gv[4 * N2 + 3], fD, a2); }
    }
    float y[8], p3 = 0.f, p2 = 0.f, p1 = 0.f;
#pragma unroll
    for (int g = 0; g < 8; ++g) {
        const f32x16& A = g < 4 ? a1 : a2;
        const int r = 4 * (g & 3);
        const float z3 = A[r + 3];
        const float z2 = A[r + 2] + dpp_shift(z3, p3);
        const float z1 = A[r + 1] + dpp_shift(z2, p2);
        y[g] = A[r] + dpp_shift(z1, p1);
        p3 = z3; p2 = z2; p1 = z1;
    }
    typedef __attribute__((address_space(3))) float* lds_f;
    if (PR < 3 && l == 63) { lds_f c = (lds_f)(lds + L2_CARRY + (ci * 4 + PR + 1) * 16); c[0] = p1; c[1] = p2; c[2] = p3; }
    unsigned* yo = (unsigned*)(ws + OFF_YY + (size_t)d * 4096 + PR * 1024) + l;
    if (PR > 0 && l < 3) { lds_f c = (lds_f)(lds + L2_PART + (ci * 4 + PR) * 32 + l * 8); c[0] = y[0]; c[1] = y[1]; }
    else yo[0] = cvtpk(y[0], y[1]);
    yo[64] = cvtpk(y[2], y[3]);
    yo[128] = cvtpk(y[4], y[5]);
    yo[192] = cvtpk(y[6], y[7]);
}

DEV void attn_slow(lds_ptr lds, int l, int w, const char* qb, const char* kvb) {
    typedef __attribute__((address_space(3))) float* lds_f;
    const int r32 = l & 31, hi = l >> 5;
    lds_f mx = (lds_f)(lds + L2_MX);
    for (int n = 0; n < 4; ++n) {
        const bf16x8 q16 = *(const bf16x8*)(qb + n * TILE + l * 16);
        s16x4 q4 = *(const s16x4*)(qb + n * TILE + 1024 + r32 * 8);
        if (hi) q4 = (s16x4){0, 0, 0, 0};
        float m = -INFINITY;
        for (int kt = 0; kt < 16; ++kt) {
            const bf16x8 k16 = *(const bf16x8*)(kvb + kt * TILE + l * 16);
            const s16x4 k4 = *(const s16x4*)(kvb + kt * TILE + 1024 + r32 * 8);
            f32x16 s = MFMA32(k16, q16, (f32x16){});
            s = MFMA32H(k4, q4, s);
#pragma unroll
            for (int r = 0; r < 16; ++r) m = fmaxf(m, s[r]);
        }
        m = fmaxf(m, __shfl_xor(m, 32));
        if (!hi) mx[w * 128 + n * 32 + r32] = m;
    }
    __syncthreads();
    for (int n = 0; n < 4; ++n) {
        const bf16x8 q16 = *(const bf16x8*)(qb + n * TILE + l * 16);
        s16x4 q4 = *(const s16x4*)(qb + n * TILE + 1024 + r32 * 8);
        if (hi) q4 = (s16x4){0, 0, 0, 0};
        const int wb = (w & 4) * 128 + n * 32 + r32;
        const float m = fmaxf(fmaxf(mx[wb], mx[wb + 128]), fmaxf(mx[wb + 256], mx[wb + 384]));
        f32x16 o = {};
        for (int kt = 0; kt < 16; ++kt) {
            const bf16x8 k16 = *(const bf16x8*)(kvb + kt * TILE + l * 16);
            const s16x4 k4 = *(const s16x4*)(kvb + kt * TILE + 1024 + r32 * 8);
            bf16x8 v0, v1;
#pragma unroll
            for (int j = 0; j < 8; ++j) { v0[j] = 0x3F80; v1[j] = 0x3F80; }
            if (r32 < 20) {
                v0 = *(const bf16x8*)(kvb + kt * TILE + 1280 + (hi * 20 + r32) * 16);
                v1 = *(const bf16x8*)(kvb + kt * TILE + 1920 + (hi * 20 + r32) * 16);
            }
            f32x16 s = MFMA32(k16, q16, (f32x16){});
            s = MFMA32H(k4, q4, s);
            u32x4 pa, pb;
#pragma unroll
            for (int r = 0; r < 4; ++r) {
                pa[r] = cvtpk(__builtin_amdgcn_exp2f(s[2 * r] - m), __builtin_amdgcn_exp2f(s[2 * r + 1] - m));
                pb[r] = cvtpk(__builtin_amdgcn_exp2f(s[8 + 2 * r] - m), __builtin_amdgcn_exp2f(s[9 + 2 * r] - m));
            }
            o = MFMA32(v0, __builtin_bit_cast(bf16x8, pa), o);
            o = MFMA32(v1, __builtin_bit_cast(bf16x8, pb), o);
        }
#pragma unroll
        for (int rg = 0; rg < 3; ++rg)
            *(__attribute__((address_space(3))) f32x4*)(lds + L2_RED + ((w * 4 + n) * 3 + rg) * 1024 + l * 16) =
                (f32x4){o[4 * rg], o[4 * rg + 1], o[4 * rg + 2], o[4 * rg + 3]};
    }
}

DEV void attn_fast(unsigned ldsdst, int l, const char* qb, const char* kvb) {
    const uint64_t qa = (uint64_t)qb, ka = (uint64_t)kvb;
    const u32x4 srdK = {(unsigned)RFL((unsigned)ka), (unsigned)RFL((unsigned)(ka >> 32)), 16u * TILE, 0x20000u};
    const u32x4 srdQ = {(unsigned)RFL((unsigned)qa), (unsigned)RFL((unsigned)(qa >> 32)), 4u * TILE, 0x20000u};
    const unsigned vo16 = l * 16, vo4 = 1024 + (l & 31) * 8, voV = 1280 + ((l >> 5) * 20 + (l & 31)) * 16, la = ldsdst + l * 16;
    unsigned so, cnt;
    asm volatile(
        "v_mov_b32 v160, 0\n\t"
        "v_mov_b32 v161, 0\n\t"
        "v_mov_b32 v162, 0\n\t"
        "v_mov_b32 v163, 0\n\t"
        "v_mov_b32 v164, 0\n\t"
        "v_mov_b32 v165, 0\n\t"
        "v_mov_b32 v166, 0\n\t"
        "v_mov_b32 v167, 0\n\t"
        "v_mov_b32 v174, 0x3f803f80\n\t"
        "v_mov_b32 v175, 0x3f803f80\n\t"
        "v_mov_b32 v176, 0x3f803f80\n\t"
        "v_mov_b32 v177, 0x3f803f80\n\t"
        "v_mov_b32 v178, 0x3f803f80\n\t"
        "v_mov_b32 v179, 0x3f803f80\n\t"
        "v_mov_b32 v180, 0x3f803f80\n\t"
        "v_mov_b32 v181, 0x3f803f80\n\t"
        "v_mov_b32 v188, 0x3f803f80\n\t"
        "v_mov_b32 v189, 0x3f803f80\n\t"
        "v_mov_b32 v190, 0x3f803f80\n\t"
        "v_mov_b32 v191, 0x3f803f80\n\t"
        "v_mov_b32 v192, 0x3f803f80\n\t"
        "v_mov_b32 v193, 0x3f803f80\n\t"
        "v_mov_b32 v194, 0x3f803f80\n\t"
        "v_mov_b32 v195, 0x3f803f80\n\t"
        "v_mov_b32 v202, 0x3f803f80\n\t"
        "v_mov_b32 v203, 0x3f803f80\n\t"
        "v_mov_b32 v204, 0x3f803f80\n\t"
        "v_mov_b32 v205, 0x3f803f80\n\t"
        "v_mov_b32 v206, 0x3f803f80\n\t"
        "v_mov_b32 v207, 0x3f803f80\n\t"
        "v_mov_b32 v208, 0x3f803f80\n\t"
        "v_mov_b32 v209, 0x3f803f80\n\t"
        "v_mov_b32 v216, 0x3f803f80\n\t"
        "v_mov_b32 v217, 0x3f803f80\n\t"
        "v_mov_b32 v218, 0x3f803f80\n\t"
        "v_mov_b32 v219, 0x3f803f80\n\t"
        "v_mov_b32 v220, 0x3f803f80\n\t"
        "v_mov_b32 v221, 0x3f803f80\n\t"
        "v_mov_b32 v222, 0x3f803f80\n\t"
        "v_mov_b32 v223, 0x3f803f80\n\t"
        "s_nop 4\n\t"
        "s_mov_b32 %0, 0\n\t"
        "buffer_load_dwordx4 v[144:147], %2, %7, %0 offen\n\t"
        "s_mov_b64 exec, %9\n\t"
        "buffer_load_dwordx2 v[160:161], %3, %7, %0 offen\n\t"
        "s_mov_b64 exec, -1\n\t"
        "s_mov_b32 %0, 3840\n\t"
        "buffer_load_dwordx4 v[148:151], %2, %7, %0 offen\n\t"
        "s_mov_b64 exec, %9\n\t"
        "buffer_load_dwordx2 v[162:163], %3, %7, %0 offen\n\t"
        "s_mov_b64 exec, -1\n\t"
        "s_mov_b32 %0, 7680\n\t"
        "buffer_load_dwordx4 v[152:155], %2, %7, %0 offen\n\t"
        "s_mov_b64 exec, %9\n\t"
        "buffer_load_dwordx2 v[164:165], %3, %7, %0 offen\n\t"
        "s_mov_b64 exec, -1\n\t"
        "s_mov_b32 %0, 11520\n\t"
        "buffer_load_dwordx4 v[156:159], %2, %7, %0 offen\n\t"
        "s_mov_b64 exec, %9\n\t"
        "buffer_load_dwordx2 v[166:167], %3, %7, %0 offen\n\t"
        "s_mov_b64 exec, -1\n\t"
        "s_mov_b32 %0, 0\n\t"
        "buffer_load_dwordx4 v[168:171], %2, %6, %0 offen\n\t"
        "buffer_load_dwordx2 v[172:173], %3, %6, %0 offen\n\t"
        "s_mov_b64 exec, %8\n\t"
        "buffer_load_dwordx4 v[174:177], %4, %6, %0 offen\n\t"
        "buffer_load_dwordx4 v[178:181], %4, %6, %0 offen offset:640\n\t"
        "s_mov_b64 exec, -1\n\t"
        "s_mov_b32 %0, 3840\n\t"
        "buffer_load_dwordx4 v[182:185], %2, %6, %0 offen\n\t"
        "buffer_load_dwordx2 v[186:187], %3, %6, %0 offen\n\t"
        "s_mov_b64 exec, %8\n\t"
        "buffer_load_dwordx4 v[188:191], %4, %6, %0 offen\n\t"
        "buffer_load_dwordx4 v[192:195], %4, %6, %0 offen offset:640\n\t"
        "s_mov_b64 exec, -1\n\t"
        "s_mov_b32 %0, 7680\n\t"
        "buffer_load_dwordx4 v[196:199], %2, %6, %0 offen\n\t"
        "buffer_load_dwordx2 v[200:201], %3, %6, %0 offen\n\t"
        "s_mov_b64 exec, %8\n\t"
        "buffer_load_dwordx4 v[202:205], %4, %6, %0 offen\n\t"
        "buffer_load_dwordx4 v[206:209], %4, %6, %0 offen offset:640\n\t"
        "s_mov_b64 exec, -1\n\t"
        "s_mov_b32 %0, 11520\n\t"
        "s_mov_b32 %1, 4\n\t"
        "v_mov_b32 v32, 0\n\t"
        "v_mov_b32 v33, 0\n\t"
        "v_mov_b32 v34, 0\n\t"
        "v_mov_b32 v35, 0\n\t"
        "v_mov_b32 v36, 0\n\t"
        "v_mov_b32 v37, 0\n\t"
        "v_mov_b32 v38, 0\n\t"
        "v_mov_b32 v39, 0\n\t"
        "v_mov_b32 v40, 0\n\t"
        "v_mov_b32 v41, 0\n\t"
        "v_mov_b32 v42, 0\n\t"
        "v_mov_b32 v43, 0\n\t"
        "v_mov_b32 v44, 0\n\t"
        "v_mov_b32 v45, 0\n\t"
        "v_mov_b32 v46, 0\n\t"
        "v_mov_b32 v47, 0\n\t"
        "v_mov_b32 v48, 0\n\t"
        "v_mov_b32 v49, 0\n\t"
        "v_mov_b32 v50, 0\n\t"
        "v_mov_b32 v51, 0\n\t"
        "v_mov_b32 v52, 0\n\t"
        "v_mov_b32 v53, 0\n\t"
        "v_mov_b32 v54, 0\n\t"
        "v_mov_b32 v55, 0\n\t"
        "v_mov_b32 v56, 0\n\t"
        "v_mov_b32 v57, 0\n\t"
        "v_mov_b32 v58, 0\n\t"
        "v_mov_b32 v59, 0\n\t"
        "v_mov_b32 v60, 0\n\t"
        "v_mov_b32 v61, 0\n\t"
        "v_mov_b32 v62, 0\n\t"
        "v_mov_b32 v63, 0\n\t"
        "v_mov_b32 v64, 0\n\t"
        "v_mov_b32 v65, 0\n\t"
        "v_mov_b32 v66, 0\n\t"
        "v_mov_b32 v67, 0\n\t"
        "v_mov_b32 v68, 0\n\t"
        "v_mov_b32 v69, 0\n\t"
        "v_mov_b32 v70, 0\n\t"
        "v_mov_b32 v71, 0\n\t"
        "v_mov_b32 v72, 0\n\t"
        "v_mov_b32 v73, 0\n\t"
        "v_mov_b32 v74, 0\n\t"
        "v_mov_b32 v75, 0\n\t"
        "v_mov_b32 v76, 0\n\t"
        "v_mov_b32 v77, 0\n\t"
        "v_mov_b32 v78, 0\n\t"
        "v_mov_b32 v79, 0\n\t"
        "v_mov_b32 v80, 0\n\t"
        "v_mov_b32 v81, 0\n\t"
        "v_mov_b32 v82, 0\n\t"
        "v_mov_b32 v83, 0\n\t"
        "v_mov_b32 v84, 0\n\t"
        "v_mov_b32 v85, 0\n\t"
        "v_mov_b32 v86, 0\n\t"
        "v_mov_b32 v87, 0\n\t"
        "v_mov_b32 v88, 0\n\t"
        "v_mov_b32 v89, 0\n\t"
        "v_mov_b32 v90, 0\n\t"
        "v_mov_b32 v91, 0\n\t"
        "v_mov_b32 v92, 0\n\t"
        "v_mov_b32 v93, 0\n\t"
        "v_mov_b32 v94, 0\n\t"
        "v_mov_b32 v95, 0\n\t"
        "v_mov_b32 v136, 0\n\t"
        "v_mov_b32 v137, 0\n\t"
        "v_mov_b32 v138, 0\n\t"
        "v_mov_b32 v139, 0\n\t"
        "v_mov_b32 v140, 0\n\t"
        "v_mov_b32 v141, 0\n\t"
        "v_mov_b32 v142, 0\n\t"
        "v_mov_b32 v143, 0\n\t"
        "s_waitcnt vmcnt(8)\n\t"
        "v_mfma_f32_32x32x16_bf16 v[96:111], v[168:171], v[144:147], 0\n\t"
        "s_nop 1\n\t"
        "v_mfma_f32_32x32x8_bf16 v[96:111], v[172:173], v[160:161], v[96:111]\n\t"
        "s_nop 7\n\t"
        "s_nop 3\n\t"
        "LOOP%=:\n\t"
        "v_mfma_f32_32x32x16_bf16 v[112:127], v[168:171], v[148:151], 0\n\t"
        "buffer_load_dwordx4 v[210:213], %2, %6, %0 offen\n\t"
        "v_exp_f32 v96, v96\n\t"
        "v_exp_f32 v97, v97\n\t"
        "v_exp_f32 v98, v98\n\t"
        "v_exp_f32 v99, v99\n\t"
        "v_exp_f32 v100, v100\n\t"
        "v_mfma_f32_32x32x16_bf16 v[80:95], v[216:219], v[136:139], v[80:95]\n\t"
        "v_exp_f32 v101, v101\n\t"
        "v_exp_f32 v102, v102\n\t"
        "v_exp_f32 v103, v103\n\t"
        "v_exp_f32 v104, v104\n\t"
        "v_exp_f32 v105, v105\n\t"
        "v_mfma_f32_32x32x8_bf16 v[112:127], v[172:173], v[162:163], v[112:127]\n\t"
        "v_exp_f32 v106, v106\n\t"
        "v_exp_f32 v107, v107\n\t"
        "v_exp_f32 v108, v108\n\t"
        "v_exp_f32 v109, v109\n\t"
        "v_exp_f32 v110, v110\n\t"
        "v_exp_f32 v111, v111\n\t"
        "v_mfma_f32_32x32x16_bf16 v[80:95], v[220:223], v[140:143], v[80:95]\n\t"
        "v_cvt_pk_bf16_f32 v128, v96, v97\n\t"
        "v_cvt_pk_bf16_f32 v129, v98, v99\n\t"
        "v_cvt_pk_bf16_f32 v130, v100, v101\n\t"
        "v_cvt_pk_bf16_f32 v131, v102, v103\n\t"
        "v_cvt_pk_bf16_f32 v132, v104, v105\n\t"
        "v_cvt_pk_bf16_f32 v133, v106, v107\n\t"
        "v_cvt_pk_bf16_f32 v134, v108, v109\n\t"
        "v_cvt_pk_bf16_f32 v135, v110, v111\n\t"
        "v_mfma_f32_32x32x16_bf16 v[96:111], v[168:171], v[152:155], 0\n\t"
        "buffer_load_dwordx2 v[214:215], %3, %6, %0 offen\n\t"
        "v_exp_f32 v112, v112\n\t"
        "v_exp_f32 v113, v113\n\t"
        "v_exp_f32 v114, v114\n\t"
        "v_exp_f32 v115, v115\n\t"
        "v_exp_f32 v116, v116\n\t"
        "v_mfma_f32_32x32x16_bf16 v[32:47], v[174:177], v[128:131], v[32:47]\n\t"
        "v_exp_f32 v117, v117\n\t"
        "v_exp_f32 v118, v118\n\t"
        "v_exp_f32 v119, v119\n\t"
        "v_exp_f32 v120, v120\n\t"
        "v_exp_f32 v121, v121\n\t"
        "v_mfma_f32_32x32x8_bf16 v[96:111], v[172:173], v[164:165], v[96:111]\n\t"
        "v_exp_f32 v122, v122\n\t"
        "v_exp_f32 v123, v123\n\t"
        "v_exp_f32 v124, v124\n\t"
        "v_exp_f32 v125, v125\n\t"
        "v_exp_f32 v126, v126\n\t"
        "v_exp_f32 v127, v127\n\t"
        "v_mfma_f32_32x32x16_bf16 v[32:47], v[178:181], v[132:135], v[32:47]\n\t"
        "v_cvt_pk_bf16_f32 v136, v112, v113\n\t"
        "v_cvt_pk_bf16_f32 v137, v114, v115\n\t"
        "v_cvt_pk_bf16_f32 v138, v116, v117\n\t"
        "v_cvt_pk_bf16_f32 v139, v118, v119\n\t"
        "v_cvt_pk_bf16_f32 v140, v120, v121\n\t"
        "v_cvt_pk_bf16_f32 v141, v122, v123\n\t"
        "v_cvt_pk_bf16_f32 v142, v124, v125\n\t"
        "v_cvt_pk_bf16_f32 v143, v126, v127\n\t"
        "v_mfma_f32_32x32x16_bf16 v[112:127], v[168:171], v[156:159], 0\n\t"
        "s_mov_b64 exec, %8\n\t"
        "buffer_load_dwordx4 v[216:219], %4, %6, %0 offen\n\t"
        "buffer_load_dwordx4 v[220:223], %4, %6, %0 offen offset:640\n\t"
        "s_mov_b64 exec, -1\n\t"
        "v_exp_f32 v96, v96\n\t"
        "v_exp_f32 v97, v97\n\t"
        "v_exp_f32 v98, v98\n\t"
        "v_exp_f32 v99, v99\n\t"
        "v_exp_f32 v100, v100\n\t"
        "v_mfma_f32_32x32x16_bf16 v[48:63], v[174:177], v[136:139], v[48:63]\n\t"
        "v_exp_f32 v101, v101\n\t"
        "v_exp_f32 v102, v102\n\t"
        "v_exp_f32 v103, v103\n\t"
        "v_exp_f32 v104, v104\n\t"
        "v_exp_f32 v105, v105\n\t"
        "v_mfma_f32_32x32x8_bf16 v[112:127], v[172:173], v[166:167], v[112:127]\n\t"
        "v_exp_f32 v106, v106\n\t"
        "v_exp_f32 v107, v107\n\t"
        "v_exp_f32 v108, v108\n\t"
        "v_exp_f32 v109, v109\n\t"
        "v_exp_f32 v110, v110\n\t"
        "v_exp_f32 v111, v111\n\t"
        "v_mfma_f32_32x32x16_bf16 v[48:63], v[178:181], v[140:143], v[48:63]\n\t"
        "v_cvt_pk_bf16_f32 v128, v96, v97\n\t"
        "v_cvt_pk_bf16_f32 v129, v98, v99\n\t"
        "v_cvt_pk_bf16_f32 v130, v100, v101\n\t"
        "v_cvt_pk_bf16_f32 v131, v102, v103\n\t"
        "v_cvt_pk_bf16_f32 v132, v104, v105\n\t"
        "v_cvt_pk_bf16_f32 v133, v106, v107\n\t"
        "v_cvt_pk_bf16_f32 v134, v108, v109\n\t"
        "v_cvt_pk_bf16_f32 v135, v110, v111\n\t"
        "s_waitcnt vmcnt(8)\n\t"
        "v_mfma_f32_32x32x16_bf16 v[96:111], v[182:185], v[144:147], 0\n\t"
        "s_add_u32 %0, %0, 3840\n\t"
        "v_exp_f32 v112, v112\n\t"
        "v_exp_f32 v113, v113\n\t"
        "v_exp_f32 v114, v114\n\t"
        "v_exp_f32 v115, v115\n\t"
        "v_exp_f32 v116, v116\n\t"
        "v_mfma_f32_32x32x16_bf16 v[64:79], v[174:177], v[128:131], v[64:79]\n\t"
        "v_exp_f32 v117, v117\n\t"
        "v_exp_f32 v118, v118\n\t"
        "v_exp_f32 v119, v119\n\t"
        "v_exp_f32 v120, v120\n\t"
        "v_exp_f32 v121, v121\n\t"
        "v_mfma_f32_32x32x8_bf16 v[96:111], v[186:187], v[160:161], v[96:111]\n\t"
        "v_exp_f32 v122, v122\n\t"
        "v_exp_f32 v123, v123\n\t"
        "v_exp_f32 v124, v124\n\t"
        "v_exp_f32 v125, v125\n\t"
        "v_exp_f32 v126, v126\n\t"
        "v_exp_f32 v127, v127\n\t"
        "v_mfma_f32_32x32x16_bf16 v[64:79], v[178:181], v[132:135], v[64:79]\n\t"
        "v_cvt_pk_bf16_f32 v136, v112, v113\n\t"
        "v_cvt_pk_bf16_f32 v137, v114, v115\n\t"
        "v_cvt_pk_bf16_f32 v138, v116, v117\n\t"
        "v_cvt_pk_bf16_f32 v139, v118, v119\n\t"
        "v_cvt_pk_bf16_f32 v140, v120, v121\n\t"
        "v_cvt_pk_bf16_f32 v141, v122, v123\n\t"
        "v_cvt_pk_bf16_f32 v142, v124, v125\n\t"
        "v_cvt_pk_bf16_f32 v143, v126, v127\n\t"
        "v_mfma_f32_32x32x16_bf16 v[112:127], v[182:185], v[148:151], 0\n\t"
        "buffer_load_dwordx4 v[168:171], %2, %6, %0 offen\n\t"
        "v_exp_f32 v96, v96\n\t"
        "v_exp_f32 v97, v97\n\t"
        "v_exp_f32 v98, v98\n\t"
        "v_exp_f32 v99, v99\n\t"
        "v_exp_f32 v100, v100\n\t"
        "v_mfma_f32_32x32x16_bf16 v[80:95], v[174:177], v[136:139], v[80:95]\n\t"
        "v_exp_f32 v101, v101\n\t"
        "v_exp_f32 v102, v102\n\t"
        "v_exp_f32 v103, v103\n\t"
        "v_exp_f32 v104, v104\n\t"
        "v_exp_f32 v105, v105\n\t"
        "v_mfma_f32_32x32x8_bf16 v[112:127], v[186:187], v[162:163], v[112:127]\n\t"
        "v_exp_f32 v106, v106\n\t"
        "v_exp_f32 v107, v107\n\t"
        "v_exp_f32 v108, v108\n\t"
        "v_exp_f32 v109, v109\n\t"
        "v_exp_f32 v110, v110\n\t"
        "v_exp_f32 v111, v111\n\t"
        "v_mfma_f32_32x32x16_bf16 v[80:95], v[178:181], v[140:143], v[80:95]\n\t"
        "v_cvt_pk_bf16_f32 v128, v96, v97\n\t"
        "v_cvt_pk_bf16_f32 v129, v98, v99\n\t"
        "v_cvt_pk_bf16_f32 v130, v100, v101\n\t"
        "v_cvt_pk_bf16_f32 v131, v102, v103\n\t"
        "v_cvt_pk_bf16_f32 v132, v104, v105\n\t"
        "v_cvt_pk_bf16_f32 v133, v106, v107\n\t"
        "v_cvt_pk_bf16_f32 v134, v108, v109\n\t"
        "v_cvt_pk_bf16_f32 v135, v110, v111\n\t"
        "v_mfma_f32_32x32x16_bf16 v[96:111], v[182:185], v[152:155], 0\n\t"
        "buffer_load_dwordx2 v[172:173], %3, %6, %0 offen\n\t"
        "v_exp_f32 v112, v112\n\t"
        "v_exp_f32 v113, v113\n\t"
        "v_exp_f32 v114, v114\n\t"
        "v_exp_f32 v115, v115\n\t"
        "v_exp_f32 v116, v116\n\t"
        "v_mfma_f32_32x32x16_bf16 v[32:47], v[188:191], v[128:131], v[32:47]\n\t"
        "v_exp_f32 v117, v117\n\t"
        "v_exp_f32 v118, v118\n\t"
        "v_exp_f32 v119, v119\n\t"
        "v_exp_f32 v120, v120\n\t"
        "v_exp_f32 v121, v121\n\t"
        "v_mfma_f32_32x32x8_bf16 v[96:111], v[186:187], v[164:165], v[96:111]\n\t"
        "v_exp_f32 v122, v122\n\t"
        "v_exp_f32 v123, v123\n\t"
        "v_exp_f32 v124, v124\n\t"
        "v_exp_f32 v125, v125\n\t"
        "v_exp_f32 v126, v126\n\t"
        "v_exp_f32 v127, v127\n\t"
        "v_mfma_f32_32x32x16_bf16 v[32:47], v[192:195], v[132:135], v[32:47]\n\t"
        "v_cvt_pk_bf16_f32 v136, v112, v113\n\t"
        "v_cvt_pk_bf16_f32 v137, v114, v115\n\t"
        "v_cvt_pk_bf16_f32 v138, v116, v117\n\t"
        "v_cvt_pk_bf16_f32 v139, v118, v119\n\t"
        "v_cvt_pk_bf16_f32 v140, v120, v121\n\t"
        "v_cvt_pk_bf16_f32 v141, v122, v123\n\t"
        "v_cvt_pk_bf16_f32 v142, v124, v125\n\t"
        "v_cvt_pk_bf16_f32 v143, v126, v127\n\t"
        "v_mfma_f32_32x32x16_bf16 v[112:127], v[182:185], v[156:159], 0\n\t"
        "s_mov_b64 exec, %8\n\t"
        "buffer_load_dwordx4 v[174:177], %4, %6, %0 offen\n\t"
        "buffer_load_dwordx4 v[178:181], %4, %6, %0 offen offset:640\n\t"
        "s_mov_b64 exec, -1\n\t"
        "v_exp_f32 v96, v96\n\t"
        "v_exp_f32 v97, v97\n\t"
        "v_exp_f32 v98, v98\n\t"
        "v_exp_f32 v99, v99\n\t"
        "v_exp_f32 v100, v100\n\t"
        "v_mfma_f32_32x32x16_bf16 v[48:63], v[188:191], v[136:139], v[48:63]\n\t"
        "v_exp_f32 v101, v101\n\t"
        "v_exp_f32 v102, v102\n\t"
        "v_exp_f32 v103, v103\n\t"
        "v_exp_f32 v104, v104\n\t"
        "v_exp_f32 v105, v105\n\t"
        "v_mfma_f32_32x32x8_bf16 v[112:127], v[186:187], v[166:167], v[112:127]\n\t"
        "v_exp_f32 v106, v106\n\t"
        "v_exp_f32 v107, v107\n\t"
        "v_exp_f32 v108, v108\n\t"
        "v_exp_f32 v109, v109\n\t"
        "v_exp_f32 v110, v110\n\t"
        "v_exp_f32 v111, v111\n\t"
        "v_mfma_f32_32x32x16_bf16 v[48:63], v[192:195], v[140:143], v[48:63]\n\t"
        "v_cvt_pk_bf16_f32 v128, v96, v97\n\t"
        "v_cvt_pk_bf16_f32 v129, v98, v99\n\t"
        "v_cvt_pk_bf16_f32 v130, v100, v101\n\t"
        "v_cvt_pk_bf16_f32 v131, v102, v103\n\t"
        "v_cvt_pk_bf16_f32 v132, v104, v105\n\t"
        "v_cvt_pk_bf16_f32 v133, v106, v107\n\t"
        "v_cvt_pk_bf16_f32 v134, v108, v109\n\t"
        "v_cvt_pk_bf16_f32 v135, v110, v111\n\t"
        "s_waitcnt vmcnt(8)\n\t"
        "v_mfma_f32_32x32x16_bf16 v[96:111], v[196:199], v[144:147], 0\n\t"
        "s_add_u32 %0, %0, 3840\n\t"
        "v_exp_f32 v112, v112\n\t"
        "v_exp_f32 v113, v113\n\t"
        "v_exp_f32 v114, v114\n\t"
        "v_exp_f32 v115, v115\n\t"
        "v_exp_f32 v116, v116\n\t"
        "v_mfma_f32_32x32x16_bf16 v[64:79], v[188:191], v[128:131], v[64:79]\n\t"
        "v_exp_f32 v117, v117\n\t"
        "v_exp_f32 v118, v118\n\t"
        "v_exp_f32 v119, v119\n\t"
        "v_exp_f32 v120, v120\n\t"
        "v_exp_f32 v121, v121\n\t"
        "v_mfma_f32_32x32x8_bf16 v[96:111], v[200:201], v[160:161], v[96:111]\n\t"
        "v_exp_f32 v122, v122\n\t"
        "v_exp_f32 v123, v123\n\t"
        "v_exp_f32 v124, v124\n\t"
        "v_exp_f32 v125, v125\n\t"
        "v_exp_f32 v126, v126\n\t"
        "v_exp_f32 v127, v127\n\t"
        "v_mfma_f32_32x32x16_bf16 v[64:79], v[192:195], v[132:135], v[64:79]\n\t"
        "v_cvt_pk_bf16_f32 v136, v112, v113\n\t"
        "v_cvt_pk_bf16_f32 v137, v114, v115\n\t"
        "v_cvt_pk_bf16_f32 v138, v116, v117\n\t"
        "v_cvt_pk_bf16_f32 v139, v118, v119\n\t"
        "v_cvt_pk_bf16_f32 v140, v120, v121\n\t"
        "v_cvt_pk_bf16_f32 v141, v122, v123\n\t"
        "v_cvt_pk_bf16_f32 v142, v124, v125\n\t"
        "v_cvt_pk_bf16_f32 v143, v126, v127\n\t"
        "v_mfma_f32_32x32x16_bf16 v[112:127], v[196:199], v[148:151], 0\n\t"
        "buffer_load_dwordx4 v[182:185], %2, %6, %0 offen\n\t"
        "v_exp_f32 v96, v96\n\t"
        "v_exp_f32 v97, v97\n\t"
        "v_exp_f32 v98, v98\n\t"
        "v_exp_f32 v99, v99\n\t"
        "v_exp_f32 v100, v100\n\t"
        "v_mfma_f32_32x32x16_bf16 v[80:95], v[188:191], v[136:139], v[80:95]\n\t"
        "v_exp_f32 v101, v101\n\t"
        "v_exp_f32 v102, v102\n\t"
        "v_exp_f32 v103, v103\n\t"
        "v_exp_f32 v104, v104\n\t"
        "v_exp_f32 v105, v105\n\t"
        "v_mfma_f32_32x32x8_bf16 v[112:127], v[200:201], v[162:163], v[112:127]\n\t"
        "v_exp_f32 v106, v106\n\t"
        "v_exp_f32 v107, v107\n\t"
        "v_exp_f32 v108, v108\n\t"
        "v_exp_f32 v109, v109\n\t"
        "v_exp_f32 v110, v110\n\t"
        "v_exp_f32 v111, v111\n\t"
        "v_mfma_f32_32x32x16_bf16 v[80:95], v[192:195], v[140:143], v[80:95]\n\t"
        "v_cvt_pk_bf16_f32 v128, v96, v97\n\t"
        "v_cvt_pk_bf16_f32 v129, v98, v99\n\t"
        "v_cvt_pk_bf16_f32 v130, v100, v101\n\t"
        "v_cvt_pk_bf16_f32 v131, v102, v103\n\t"
        "v_cvt_pk_bf16_f32 v132, v104, v105\n\t"
        "v_cvt_pk_bf16_f32 v133, v106, v107\n\t"
        "v_cvt_pk_bf16_f32 v134, v108, v109\n\t"
        "v_cvt_pk_bf16_f32 v135, v110, v111\n\t"
        "v_mfma_f32_32x32x16_bf16 v[96:111], v[196:199], v[152:155], 0\n\t"
        "buffer_load_dwordx2 v[186:187], %3, %6, %0 offen\n\t"
        "v_exp_f32 v112, v112\n\t"
        "v_exp_f32 v113, v113\n\t"
        "v_exp_f32 v114, v114\n\t"
        "v_exp_f32 v115, v115\n\t"
        "v_exp_f32 v116, v116\n\t"
        "v_mfma_f32_32x32x16_bf16 v[32:47], v[202:205], v[128:131], v[32:47]\n\t"
        "v_exp_f32 v117, v117\n\t"
        "v_exp_f32 v118, v118\n\t"
        "v_exp_f32 v119, v119\n\t"
        "v_exp_f32 v120, v120\n\t"
        "v_exp_f32 v121, v121\n\t"
        "v_mfma_f32_32x32x8_bf16 v[96:111], v[200:201], v[164:165], v[96:111]\n\t"
        "v_exp_f32 v122, v122\n\t"
        "v_exp_f32 v123, v123\n\t"
        "v_exp_f32 v124, v124\n\t"
        "v_exp_f32 v125, v125\n\t"
        "v_exp_f32 v126, v126\n\t"
        "v_exp_f32 v127, v127\n\t"
        "v_mfma_f32_32x32x16_bf16 v[32:47], v[206:209], v[132:135], v[32:47]\n\t"
        "v_cvt_pk_bf16_f32 v136, v112, v113\n\t"
        "v_cvt_pk_bf16_f32 v137, v114, v115\n\t"
        "v_cvt_pk_bf16_f32 v138, v116, v117\n\t"
        "v_cvt_pk_bf16_f32 v139, v118, v119\n\t"
        "v_cvt_pk_bf16_f32 v140, v120, v121\n\t"
        "v_cvt_pk_bf16_f32 v141, v122, v123\n\t"
        "v_cvt_pk_bf16_f32 v142, v124, v125\n\t"
        "v_cvt_pk_bf16_f32 v143, v126, v127\n\t"
        "v_mfma_f32_32x32x16_bf16 v[112:127], v[196:199], v[156:159], 0\n\t"
        "s_mov_b64 exec, %8\n\t"
        "buffer_load_dwordx4 v[188:191], %4, %6, %0 offen\n\t"
        "buffer_load_dwordx4 v[192:195], %4, %6, %0 offen offset:640\n\t"
        "s_mov_b64 exec, -1\n\t"
        "v_exp_f32 v96, v96\n\t"
        "v_exp_f32 v97, v97\n\t"
        "v_exp_f32 v98, v98\n\t"
        "v_exp_f32 v99, v99\n\t"
        "v_exp_f32 v100, v100\n\t"
        "v_mfma_f32_32x32x16_bf16 v[48:63], v[202:205], v[136:139], v[48:63]\n\t"
        "v_exp_f32 v101, v101\n\t"
        "v_exp_f32 v102, v102\n\t"
        "v_exp_f32 v103, v103\n\t"
        "v_exp_f32 v104, v104\n\t"
        "v_exp_f32 v105, v105\n\t"
        "v_mfma_f32_32x32x8_bf16 v[112:127], v[200:201], v[166:167], v[112:127]\n\t"
        "v_exp_f32 v106, v106\n\t"
        "v_exp_f32 v107, v107\n\t"
        "v_exp_f32 v108, v108\n\t"
        "v_exp_f32 v109, v109\n\t"
        "v_exp_f32 v110, v110\n\t"
        "v_exp_f32 v111, v111\n\t"
        "v_mfma_f32_32x32x16_bf16 v[48:63], v[206:209], v[140:143], v[48:63]\n\t"
        "v_cvt_pk_bf16_f32 v128, v96, v97\n\t"
        "v_cvt_pk_bf16_f32 v129, v98, v99\n\t"
        "v_cvt_pk_bf16_f32 v130, v100, v101\n\t"
        "v_cvt_pk_bf16_f32 v131, v102, v103\n\t"
        "v_cvt_pk_bf16_f32 v132, v104, v105\n\t"
        "v_cvt_pk_bf16_f32 v133, v106, v107\n\t"
        "v_cvt_pk_bf16_f32 v134, v108, v109\n\t"
        "v_cvt_pk_bf16_f32 v135, v110, v111\n\t"
        "s_waitcnt vmcnt(8)\n\t"
        "v_mfma_f32_32x32x16_bf16 v[96:111], v[210:213], v[144:147], 0\n\t"
        "s_add_u32 %0, %0, 3840\n\t"
        "v_exp_f32 v112, v112\n\t"
        "v_exp_f32 v113, v113\n\t"
        "v_exp_f32 v114, v114\n\t"
        "v_exp_f32 v115, v115\n\t"
        "v_exp_f32 v116, v116\n\t"
        "v_mfma_f32_32x32x16_bf16 v[64:79], v[202:205], v[128:131], v[64:79]\n\t"
        "v_exp_f32 v117, v117\n\t"
        "v_exp_f32 v118, v118\n\t"
        "v_exp_f32 v119, v119\n\t"
        "v_exp_f32 v120, v120\n\t"
        "v_exp_f32 v121, v121\n\t"
        "v_mfma_f32_32x32x8_bf16 v[96:111], v[214:215], v[160:161], v[96:111]\n\t"
        "v_exp_f32 v122, v122\n\t"
        "v_exp_f32 v123, v123\n\t"
        "v_exp_f32 v124, v124\n\t"
        "v_exp_f32 v125, v125\n\t"
        "v_exp_f32 v126, v126\n\t"
        "v_exp_f32 v127, v127\n\t"
        "v_mfma_f32_32x32x16_bf16 v[64:79], v[206:209], v[132:135], v[64:79]\n\t"
        "v_cvt_pk_bf16_f32 v136, v112, v113\n\t"
        "v_cvt_pk_bf16_f32 v137, v114, v115\n\t"
        "v_cvt_pk_bf16_f32 v138, v116, v117\n\t"
        "v_cvt_pk_bf16_f32 v139, v118, v119\n\t"
        "v_cvt_pk_bf16_f32 v140, v120, v121\n\t"
        "v_cvt_pk_bf16_f32 v141, v122, v123\n\t"
        "v_cvt_pk_bf16_f32 v142, v124, v125\n\t"
        "v_cvt_pk_bf16_f32 v143, v126, v127\n\t"
        "v_mfma_f32_32x32x16_bf16 v[112:127], v[210:213], v[148:151], 0\n\t"
        "buffer_load_dwordx4 v[196:199], %2, %6, %0 offen\n\t"
        "v_exp_f32 v96, v96\n\t"
        "v_exp_f32 v97, v97\n\t"
        "v_exp_f32 v98, v98\n\t"
        "v_exp_f32 v99, v99\n\t"
        "v_exp_f32 v100, v100\n\t"
        "v_mfma_f32_32x32x16_bf16 v[80:95], v[202:205], v[136:139], v[80:95]\n\t"
        "v_exp_f32 v101, v101\n\t"
        "v_exp_f32 v102, v102\n\t"
        "v_exp_f32 v103, v103\n\t"
        "v_exp_f32 v104, v104\n\t"
        "v_exp_f32 v105, v105\n\t"
        "v_mfma_f32_32x32x8_bf16 v[112:127], v[214:215], v[162:163], v[112:127]\n\t"
        "v_exp_f32 v106, v106\n\t"
        "v_exp_f32 v107, v107\n\t"
        "v_exp_f32 v108, v108\n\t"
        "v_exp_f32 v109, v109\n\t"
        "v_exp_f32 v110, v110\n\t"
        "v_exp_f32 v111, v111\n\t"
        "v_mfma_f32_32x32x16_bf16 v[80:95], v[206:209], v[140:143], v[80:95]\n\t"
        "v_cvt_pk_bf16_f32 v128, v96, v97\n\t"
        "v_cvt_pk_bf16_f32 v129, v98, v99\n\t"
        "v_cvt_pk_bf16_f32 v130, v100, v101\n\t"
        "v_cvt_pk_bf16_f32 v131, v102, v103\n\t"
        "v_cvt_pk_bf16_f32 v132, v104, v105\n\t"
        "v_cvt_pk_bf16_f32 v133, v106, v107\n\t"
        "v_cvt_pk_bf16_f32 v134, v108, v109\n\t"
        "v_cvt_pk_bf16_f32 v135, v110, v111\n\t"
        "v_mfma_f32_32x32x16_bf16 v[96:111], v[210:213], v[152:155], 0\n\t"
        "buffer_load_dwordx2 v[200:201], %3, %6, %0 offen\n\t"
        "v_exp_f32 v112, v112\n\t"
        "v_exp_f32 v113, v113\n\t"
        "v_exp_f32 v114, v114\n\t"
        "v_exp_f32 v115, v115\n\t"
        "v_exp_f32 v116, v116\n\t"
        "v_mfma_f32_32x32x16_bf16 v[32:47], v[216:219], v[128:131], v[32:47]\n\t"
        "v_exp_f32 v117, v117\n\t"
        "v_exp_f32 v118, v118\n\t"
        "v_exp_f32 v119, v119\n\t"
        "v_exp_f32 v120, v120\n\t"
        "v_exp_f32 v121, v121\n\t"
        "v_mfma_f32_32x32x8_bf16 v[96:111], v[214:215], v[164:165], v[96:111]\n\t"
        "v_exp_f32 v122, v122\n\t"
        "v_exp_f32 v123, v123\n\t"
        "v_exp_f32 v124, v124\n\t"
        "v_exp_f32 v125, v125\n\t"
        "v_exp_f32 v126, v126\n\t"
        "v_exp_f32 v127, v127\n\t"
        "v_mfma_f32_32x32x16_bf16 v[32:47], v[220:223], v[132:135], v[32:47]\n\t"
        "v_cvt_pk_bf16_f32 v136, v112, v113\n\t"
        "v_cvt_pk_bf16_f32 v137, v114, v115\n\t"
        "v_cvt_pk_bf16_f32 v138, v116, v117\n\t"
        "v_cvt_pk_bf16_f32 v139, v118, v119\n\t"
        "v_cvt_pk_bf16_f32 v140, v120, v121\n\t"
        "v_cvt_pk_bf16_f32 v141, v122, v123\n\t"
        "v_cvt_pk_bf16_f32 v142, v124, v125\n\t"
        "v_cvt_pk_bf16_f32 v143, v126, v127\n\t"
        "v_mfma_f32_32x32x16_bf16 v[112:127], v[210:213], v[156:159], 0\n\t"
        "s_mov_b64 exec, %8\n\t"
        "buffer_load_dwordx4 v[202:205], %4, %6, %0 offen\n\t"
        "buffer_load_dwordx4 v[206:209], %4, %6, %0 offen offset:640\n\t"
        "s_mov_b64 exec, -1\n\t"
        "v_exp_f32 v96, v96\n\t"
        "v_exp_f32 v97, v97\n\t"
        "v_exp_f32 v98, v98\n\t"
        "v_exp_f32 v99, v99\n\t"
        "v_exp_f32 v100, v100\n\t"
        "v_mfma_f32_32x32x16_bf16 v[48:63], v[216:219], v[136:139], v[48:63]\n\t"
        "v_exp_f32 v101, v101\n\t"
        "v_exp_f32 v102, v102\n\t"
        "v_exp_f32 v103, v103\n\t"
        "v_exp_f32 v104, v104\n\t"
        "v_exp_f32 v105, v105\n\t"
        "v_mfma_f32_32x32x8_bf16 v[112:127], v[214:215], v[166:167], v[112:127]\n\t"
        "v_exp_f32 v106, v106\n\t"
        "v_exp_f32 v107, v107\n\t"
        "v_exp_f32 v108, v108\n\t"
        "v_exp_f32 v109, v109\n\t"
        "v_exp_f32 v110, v110\n\t"
        "v_exp_f32 v111, v111\n\t"
        "v_mfma_f32_32x32x16_bf16 v[48:63], v[220:223], v[140:143], v[48:63]\n\t"
        "v_cvt_pk_bf16_f32 v128, v96, v97\n\t"
        "v_cvt_pk_bf16_f32 v129, v98, v99\n\t"
        "v_cvt_pk_bf16_f32 v130, v100, v101\n\t"
        "v_cvt_pk_bf16_f32 v131, v102, v103\n\t"
        "v_cvt_pk_bf16_f32 v132, v104, v105\n\t"
        "v_cvt_pk_bf16_f32 v133, v106, v107\n\t"
        "v_cvt_pk_bf16_f32 v134, v108, v109\n\t"
        "v_cvt_pk_bf16_f32 v135, v110, v111\n\t"
        "s_waitcnt vmcnt(8)\n\t"
        "v_mfma_f32_32x32x16_bf16 v[96:111], v[168:171], v[144:147], 0\n\t"
        "s_add_u32 %0, %0, 3840\n\t"
        "s_sub_u32 %1, %1, 1\n\t"
        "s_cmp_lg_u32 %1, 0\n\t"
        "v_exp_f32 v112, v112\n\t"
        "v_exp_f32 v113, v113\n\t"
        "v_exp_f32 v114, v114\n\t"
        "v_exp_f32 v115, v115\n\t"
        "v_exp_f32 v116, v116\n\t"
        "v_mfma_f32_32x32x16_bf16 v[64:79], v[216:219], v[128:131], v[64:79]\n\t"
        "v_exp_f32 v117, v117\n\t"
        "v_exp_f32 v118, v118\n\t"
        "v_exp_f32 v119, v119\n\t"
        "v_exp_f32 v120, v120\n\t"
        "v_exp_f32 v121, v121\n\t"
        "v_mfma_f32_32x32x8_bf16 v[96:111], v[172:173], v[160:161], v[96:111]\n\t"
        "v_exp_f32 v122, v122\n\t"
        "v_exp_f32 v123, v123\n\t"
        "v_exp_f32 v124, v124\n\t"
        "v_exp_f32 v125, v125\n\t"
        "v_exp_f32 v126, v126\n\t"
        "v_exp_f32 v127, v127\n\t"
        "v_mfma_f32_32x32x16_bf16 v[64:79], v[220:223], v[132:135], v[64:79]\n\t"
        "v_cvt_pk_bf16_f32 v136, v112, v113\n\t"
        "v_cvt_pk_bf16_f32 v137, v114, v115\n\t"
        "v_cvt_pk_bf16_f32 v138, v116, v117\n\t"
        "v_cvt_pk_bf16_f32 v139, v118, v119\n\t"
        "v_cvt_pk_bf16_f32 v140, v120, v121\n\t"
        "v_cvt_pk_bf16_f32 v141, v122, v123\n\t"
        "v_cvt_pk_bf16_f32 v142, v124, v125\n\t"
        "v_cvt_pk_bf16_f32 v143, v126, v127\n\t"
        "s_cbranch_scc1 LOOP%=\n\t"
        "s_nop 1\n\t"
        "v_mfma_f32_32x32x16_bf16 v[80:95], v[216:219], v[136:139], v[80:95]\n\t"
        "v_mfma_f32_32x32x16_bf16 v[80:95], v[220:223], v[140:143], v[80:95]\n\t"
        "ds_write_b128 %5, v[32:35] offset:0\n\t"
        "ds_write_b128 %5, v[36:39] offset:1024\n\t"
        "ds_write_b128 %5, v[40:43] offset:2048\n\t"
        "ds_write_b128 %5, v[48:51] offset:3072\n\t"
        "ds_write_b128 %5, v[52:55] offset:4096\n\t"
        "ds_write_b128 %5, v[56:59] offset:5120\n\t"
        "ds_write_b128 %5, v[64:67] offset:6144\n\t"
        "ds_write_b128 %5, v[68:71] offset:7168\n\t"
        "ds_write_b128 %5, v[72:75] offset:8192\n\t"
        "s_nop 7\n\t"
        "ds_write_b128 %5, v[80:83] offset:9216\n\t"
        "ds_write_b128 %5, v[84:87] offset:10240\n\t"
        "ds_write_b128 %5, v[88:91] offset:11264\n\t"
        "s_waitcnt vmcnt(0) lgkmcnt(0)\n\t"
        : "=&s"(so), "=&s"(cnt)
        : "v"(vo16), "v"(vo4), "v"(voV), "v"(la), "s"(srdK), "s"(srdQ), "s"(0x000FFFFF000FFFFFull), "s"(0x00000000FFFFFFFFull)
        : "memory", "scc", "v32", "v33", "v34", "v35", "v36", "v37", "v38", "v39", "v40", "v41", "v42", "v43", "v44", "v45", "v46", "v47", "v48", "v49", "v50", "v51", "v52", "v53", "v54", "v55", "v56", "v57", "v58", "v59", "v60", "v61", "v62", "v63", "v64", "v65", "v66", "v67", "v68", "v69", "v70", "v71", "v72", "v73", "v74", "v75", "v76", "v77", "v78", "v79", "v80", "v81", "v82", "v83", "v84", "v85", "v86", "v87", "v88", "v89", "v90", "v91", "v92", "v93", "v94", "v95", "v96", "v97", "v98", "v99", "v100", "v101", "v102", "v103", "v104", "v105", "v106", "v107", "v108", "v109", "v110", "v111", "v112", "v113", "v114", "v115", "v116", "v117", "v118", "v119", "v120", "v121", "v122", "v123", "v124", "v125", "v126", "v127", "v128", "v129", "v130", "v131", "v132", "v133", "v134", "v135", "v136", "v137", "v138", "v139", "v140", "v141", "v142", "v143", "v144", "v145", "v146", "v147", "v148", "v149", "v150", "v151", "v152", "v153", "v154", "v155", "v156", "v157", "v158", "v159", "v160", "v161", "v162", "v163", "v164", "v165", "v166", "v167", "v168", "v169", "v170", "v171", "v172", "v173", "v174", "v175", "v176", "v177", "v178", "v179", "v180", "v181", "v182", "v183", "v184", "v185", "v186", "v187", "v188", "v189", "v190", "v191", "v192", "v193", "v194", "v195", "v196", "v197", "v198", "v199", "v200", "v201", "v202", "v203", "v204", "v205", "v206", "v207", "v208", "v209", "v210", "v211", "v212", "v213", "v214", "v215", "v216", "v217", "v218", "v219", "v220", "v221", "v222", "v223");
}

DEV bool attn_finish(lds_ptr lds, int l, int w, int i, int p, char* ws) {
    const int hd = w >> 2, n = w & 3, hi = l >> 5;
    f32x4 o[3];
#pragma unroll
    for (int rg = 0; rg < 3; ++rg) {
        o[rg] = (f32x4){0.f, 0.f, 0.f, 0.f};
#pragma unroll
        for (int kq = 0; kq < 4; ++kq) o[rg] += *(const __attribute__((address_space(3))) f32x4*)(lds + L2_RED + (((hd * 4 + kq) * 4 + n) * 3 + rg) * 1024 + l * 16);
    }
    const float lsum = __shfl(o[2][0], (l & 31) + 32);
    const float rl = 1.0f / lsum;
    float chk = 0.f;
#pragma unroll
    for (int rg = 0; rg < 3; ++rg)
#pragma unroll
        for (int e = 0; e < 4; ++e) chk += fabsf(o[rg][e]);
    const bool bad = !(lsum > 9.765625e-4f && lsum < 1.0e12f && chk < 1.0e30f);
    char* ob = ws + OFF_O + (size_t)(128 * i + 32 * n + (l & 31)) * 1280 + (2 * p + hd) * 40 + hi * 8;
#pragma unroll
    for (int rg = 0; rg < 3; ++rg)
        if (rg < 2 || !hi) *(u32x2*)(ob + rg * 16) = (u32x2){cvtpk(o[rg][0] * rl, o[rg][1] * rl), cvtpk(o[rg][2] * rl, o[rg][3] * rl)};
    return bad;
}

__global__ __launch_bounds__(512, 2) void k2_attn(const u16* __restrict__ x, const u16* __restrict__ gw, const u16* __restrict__ ssmk,
                                                  const u16* __restrict__ ssmD, char* __restrict__ ws) {
    __shared__ __attribute__((aligned(16))) char lds_[L2_BYTES];
    lds_ptr lds = (lds_ptr)lds_;
    const int tid = threadIdx.x, l = tid & 63, w = RFL(tid >> 6);
    const int b = blockIdx.x, xc = b & 7, wi = b >> 3;
    const int i = 8 * (xc >> 2) + (wi & 7), p = 4 * (xc & 3) + (wi >> 3);
    const unsigned lds0 = (unsigned)(uintptr_t)lds_;
    const int nch = b < 128 ? 3 : 2;
    TS(1, 0);
    for (int ci = 0; ci < nch; ++ci) {
        const int d = b + 256 * ci;
        if (w < 4) glds16((const char*)ssmk + (size_t)d * 4096 + w * 1024, l * 16, lds0 + L2_CV + ci * CV_CH + 64 + w * 1024);
        else glds16(ws + OFF_U + (size_t)d * 4096 + (w - 4) * 1024, l * 16, lds0 + L2_CV + ci * CV_CH + CV_H + ((w - 4) >> 1) * CV_UH + 256 + (w & 1) * 1024);
    }
    if (tid < 3 * 36) {
        const int ci = tid / 36, e = tid - 36 * ci;
        const int off = e < 4 ? e * 16 : (e < 20 ? CV_H + (e - 4) * 16 : CV_H + CV_UH + (e - 20) * 16);
        *(__attribute__((address_space(3))) u32x4*)(lds + L2_CV + ci * CV_CH + off) = (u32x4){0, 0, 0, 0};
    }
    {
        const int t = 128 * i + 8 * p + w;
        const unsigned* xr = (const unsigned*)(x + (size_t)t * 640) + l;
        const unsigned* gr = (const unsigned*)gw + l;
        float s = 0.f;
#pragma unroll
        for (int j = 0; j < 5; ++j) { const unsigned a = xr[64 * j], c = gr[64 * j]; s += bflo(a) * bflo(c) + bfhi(a) * bfhi(c); }
#pragma unroll
        for (int o = 32; o; o >>= 1) s += __shfl_xor(s, o);
        if (l == 0) ((float*)(ws + OFF_G))[t] = 1.0f / (1.0f + __expf(-s));
    }
    asm volatile("s_waitcnt vmcnt(0) lgkmcnt(0)\n\ts_barrier" ::: "memory");
    TS(1, 1);
    {
        unsigned lst;
        if (nch == 3) lst = w == 0 ? 0xF3 : w == 1 ? 0xF7 : w == 2 ? 0xFB : w == 3 ? 0xF0A : w == 4 ? 0xF2 : w == 5 ? 0xF6 : w == 6 ? 0xF51 : 0xF849;
        else lst = w == 0 ? 0xF3 : w == 1 ? 0xF7 : w == 2 ? 0xF2 : w == 3 ? 0xF6 : w == 4 ? 0xF0 : w == 5 ? 0xF4 : w == 6 ? 0xF1 : 0xF5;
        for (; (lst & 15) != 15; lst >>= 4) {
            const int ci = (lst & 15) >> 2, pr = lst & 3, d = b + 256 * ci;
            const short Dv = (short)ssmD[d];
            bf16x8 fD;
#pragma unroll
            for (int e = 0; e < 8; ++e) fD[e] = (l == 4 * e) ? Dv : (short)0;
            if (pr == 3) conv_pair<3>(lds, l, ci, d, fD, ws);
            else if (pr == 2) conv_pair<2>(lds, l, ci, d, fD, ws);
            else if (pr == 1) conv_pair<1>(lds, l, ci, d, fD, ws);
            else conv_pair<0>(lds, l, ci, d, fD, ws);
        }
    }
    TS(1, 2);
    const int hd = 2 * p + (w >> 2);
    const char* qb = ws + OFF_QKV + (size_t)(hd * 64 + 4 * i) * TILE + 2560;
    const char* kvb = ws + OFF_QKV + (size_t)(hd * 64 + 16 * (w & 3)) * TILE;
    attn_fast(lds0 + L2_RED + w * 12288, l, qb, kvb);
    TS(1, 3);
    __syncthreads();
    TS(1, 4);
    if (tid < nch * 9) {
        const int ci = tid / 9, r = tid - 9 * ci, pr = 1 + r / 3, e = r % 3;
        typedef __attribute__((address_space(3))) float* lds_f;
        const lds_f pt = (lds_f)(lds + L2_PART + (ci * 4 + pr) * 32 + e * 8);
        const float c = ((lds_f)(lds + L2_CARRY + (ci * 4 + pr) * 16))[e];
        ((unsigned*)(ws + OFF_YY + (size_t)(b + 256 * ci) * 4096 + pr * 1024))[e] = cvtpk(pt[0] + c, pt[1]);
    }
    const bool bad = attn_finish(lds, l, w, i, p, ws);
    if (__syncthreads_or(bad)) {
        attn_slow(lds, l, w, qb, kvb);
        __syncthreads();
        attn_finish(lds, l, w, i, p, ws);
    }
    TS(1, 5);
}

constexpr int K3_OS = 16384, K3_SLOT = 36864, K3_YY = 4 * K3_SLOT;

__global__ __launch_bounds__(512, 2) void k3_out(const u16* __restrict__ x, const u16* __restrict__ wo, char* __restrict__ ws, u16* __restrict__ out) {
    __shared__ __attribute__((aligned(16))) char lds_[K3_YY + 10240];
    lds_cptr lds = (lds_cptr)lds_;
    const int tid = threadIdx.x, l = tid & 63, w = RFL(tid >> 6);
    const int b = blockIdx.x, xc = b & 7, wi = b >> 3;
    const int tt = 8 * (xc >> 1) + (wi & 7), nb = 4 * (xc & 1) + (wi >> 3);
    const int tbase = 128 * (tt >> 1) + 32 * (tt & 1);
    const unsigned lds0 = (unsigned)(uintptr_t)lds_;
    if (w >= 4) {
        const int hw = w - 4;
        unsigned ovo[4], wvo[5];
#pragma unroll
        for (int n = 0; n < 4; ++n) {
            const int c = 4 * n + (l >> 4);
            const int t = tbase + 64 * (c & 1) + 8 * hw + (c >> 1);
            ovo[n] = t * 1280 + (((l & 15) ^ c) * 16);
        }
#pragma unroll
        for (int n = 0; n < 5; ++n) {
            const int P = 64 * (5 * hw + n) + l, rho = P / 10, pc = P - 10 * rho;
            const int k = (rho & ~12) | ((rho & 4) << 1) | ((rho & 8) >> 1);
            wvo[n] = k * 1280 + 160 * nb + pc * 16;
        }
        auto issue = [&](int c) {
            const unsigned sl = lds0 + (c & 3) * K3_SLOT;
#pragma unroll
            for (int n = 0; n < 4; ++n) glds16(ws + OFF_O + c * 256, ovo[n], sl + (16 * hw + 4 * n) * 256);
#pragma unroll
            for (int n = 0; n < 5; ++n) glds16((const char*)wo + c * 128 * 1280, wvo[n], sl + K3_OS + (5 * hw + n) * 1024);
        };
        issue(0); issue(1); issue(2); issue(3);
#pragma unroll
        for (int n = 0; n < 3; ++n) {
            const int np = min(3 * hw + n, 9);
            glds16(ws + OFF_YY, (80 * nb + 8 * np + (l >> 3)) * 4096 + tt * 128 + (l & 7) * 16, lds0 + K3_YY + np * 1024);
        }
        wait_vm_bar<30>();
        wait_vm_bar<21>();
        issue(4);
        wait_vm_bar<21>();
        wait_vm_bar<12>();
        wait_vm_bar<0>();
        return;
    }
    const int c16 = l & 15, G = l >> 4, q = c16 >> 2, pp = l & 3;
    const int t = tbase + 64 * (c16 & 1) + 8 * w + (c16 >> 1);
    u32x2 xv[5];
#pragma unroll
    for (int nt = 0; nt < 5; ++nt) xv[nt] = *(const u32x2*)(x + (size_t)t * 640 + 80 * nb + 16 * nt + 4 * G);
    const float g = ((const float*)(ws + OFF_G))[t];
    const int oo = (16 * w + c16) * 256;
    const int wo_ = K3_OS + (16 * (G >> 1) + 4 * (G & 1) + q) * 160 + pp * 8;
    f32x4 acc[5];
#pragma unroll
    for (int nt = 0; nt < 5; ++nt) acc[nt] = (f32x4){0.f, 0.f, 0.f, 0.f};
    bf16x8 of[2], wf[2][5];
    auto ldfr = [&](int st, bf16x8& o, bf16x8* wv) {
        const int c = st >> 2, ks = st & 3;
        lds_cptr sl = lds + (c & 3) * K3_SLOT;
        o = ld128(sl + oo + (((4 * ks + G) ^ c16) * 16));
#pragma unroll
        for (int nt = 0; nt < 5; ++nt) wv[nt] = ldtr2(sl + wo_ + ks * 5120 + nt * 32, sl + wo_ + ks * 5120 + 1280 + nt * 32);
    };
    TS(2, 0);
    wait_lgkm_bar();
    TS(2, 1);
    ldfr(0, of[0], wf[0]);
#pragma unroll
    for (int st = 0; st < 20; ++st) {
        if (st + 1 < 20) {
            if (((st + 1) & 3) == 0) wait_lgkm_bar();
            ldfr(st + 1, of[(st + 1) & 1], wf[(st + 1) & 1]);
        }
#pragma unroll
        for (int nt = 0; nt < 5; ++nt) acc[nt] = MFMA16(wf[st & 1][nt], of[st & 1], acc[nt]);
    }
    TS(2, 2);
    const float g1 = 1.0f - g;
#pragma unroll
    for (int nt = 0; nt < 5; ++nt) {
        const u32x2 yv = __builtin_bit_cast(u32x2, ldtr(lds + K3_YY + (16 * nt + 4 * G + q) * 128 + 32 * w + 8 * pp));
        const float r0 = g * acc[nt][0] + g1 * bflo(xv[nt][0]) * bflo(yv[0]);
        const float r1 = g * acc[nt][1] + g1 * bfhi(xv[nt][0]) * bfhi(yv[0]);
        const float r2 = g * acc[nt][2] + g1 * bflo(xv[nt][1]) * bflo(yv[1]);
        const float r3 = g * acc[nt][3] + g1 * bfhi(xv[nt][1]) * bfhi(yv[1]);
        *(u32x2*)(out + (size_t)t * 640 + 80 * nb + 16 * nt + 4 * G) = (u32x2){cvtpk(r0, r1), cvtpk(r2, r3)};
    }
    TS(2, 3);
}

extern "C" void kernel_launch(void* const* d_in, const int* in_sizes, int n_in, void* d_out, int out_size, void* d_ws, size_t ws_size,
                              hipStream_t stream) {
    const u16* x = (const u16*)d_in[0];
    char* ws = (char*)d_ws;
    k1_qkv<<<256, 512, 0, stream>>>(x, (const u16*)d_in[1], (const u16*)d_in[4], (const u16*)d_in[5], (const u16*)d_in[6], ws);
    k2_attn<<<256, 512, 0, stream>>>(x, (const u16*)d_in[9], (const u16*)d_in[7], (const u16*)d_in[8], ws);
    k3_out<<<256, 512, 0, stream>>>(x, (const u16*)d_in[2], ws, (u16*)d_out);
}
```

```cpp
#define FORCE_SLOW 0
#define TSTAMP 0
#define PUBB 3
#define NPRE 24
#define FLK 6
#define NPER 7
#define PSL 2
#define CODEPF 30900
#define TOUCH 0
#define PRT 8
#define HK -1
#define UK 9
#define HPF 0
#define EXPT 0
#define NPOLL 2
#define FS1V 128
#define F1M 0
#define FOM 0
#define OST 16
#define OWIDE 1
#define EV1 1
#define EDN 1
#define SL0 4
#define SL1 1
#define ONEP 3
#define EARLY 0
#define QST 16
#define FB 1
#define ST2 1
#define HR 0
#define LFY 0
#include <hip/hip_runtime.h>
#include <hip/hip_bf16.h>
#include <stdint.h>

typedef unsigned short u16;
typedef unsigned long long u64;
typedef __attribute__((ext_vector_type(8))) short bf16x8;
typedef __attribute__((ext_vector_type(4))) short s16x4;
typedef __attribute__((ext_vector_type(16))) float f32x16;
typedef __attribute__((ext_vector_type(4))) float f32x4;
typedef __attribute__((ext_vector_type(4))) unsigned u32x4;
typedef __attribute__((ext_vector_type(2))) unsigned u32x2;
typedef __attribute__((ext_vector_type(2))) float f32x2;
typedef __attribute__((ext_vector_type(2))) __bf16 bf16x2;
typedef __attribute__((address_space(3))) const char* lds_cptr;
typedef __attribute__((address_space(3))) char* lds_ptr;
typedef __attribute__((address_space(3))) unsigned* lds_u32;
typedef __attribute__((address_space(3))) float* lds_f;
typedef __attribute__((address_space(1))) u64 gu64;
typedef __amdgpu_buffer_rsrc_t rsrc_t;

#define MFMA32(a, b, c) __builtin_amdgcn_mfma_f32_32x32x16_bf16(a, b, c, 0, 0, 0)
#define MFMA32H(a, b, c) __builtin_amdgcn_mfma_f32_32x32x8bf16_1k(a, b, c, 0, 0, 0)
#define MFMA16(a, b, c) __builtin_amdgcn_mfma_f32_16x16x32_bf16(a, b, c, 0, 0, 0)
#define RFL(x) __builtin_amdgcn_readfirstlane(x)
#define DEV __device__ __forceinline__
#define RLX_AGENT __ATOMIC_RELAXED, __HIP_MEMORY_SCOPE_AGENT
#define RLX_WG __ATOMIC_RELAXED, __HIP_MEMORY_SCOPE_WORKGROUP

constexpr unsigned PS = 0x80000;
constexpr unsigned OFF_Q = 0x50000;
constexpr unsigned OFF_S = 8u << 20, SS = 1u << 20;
constexpr unsigned OFF_U = OFF_S;
constexpr unsigned OFF_YY = OFF_S + 0x50000;
constexpr unsigned OFF_O = OFF_S + 0xA0000;
constexpr unsigned OFF_CARRY = OFF_S + 0xFD000;
constexpr unsigned OFF_F1 = OFF_S + 0xF0000;
constexpr unsigned OFF_F2 = OFF_S + 0xFE800;
constexpr unsigned OFF_FY = OFF_S + 0xF8800;
constexpr unsigned OFF_FO = OFF_S + 0xF9000;
constexpr unsigned OFF_G = OFF_S + 0xFA000;
constexpr unsigned OFF_EP = OFF_S + 0xFC000;
constexpr unsigned WS_USED = 16u << 20;
constexpr int FS1 = FS1V, FS2 = 8, FSY = 8, FSO = 8;
#define F1(gi, gj) (F1M ? (gi) * PS + 0x78000 + (gj) * FS1 : OFF_F1 + ((gi) * 16 + (gj)) * FS1)
#define FO(X, i, p) (FOM ? (i) * PS + 0x78000 + (p) * FS1 + (FS1 / 4) * (1 + (X)) : OFF_FO + ((X) * 256 + (i) * 16 + (p)) * FSO)
constexpr int KVT = 5120, QT = 2560;
constexpr float QSCALE = 0.22360679774997896f * 1.4426950408889634f;

constexpr int CV_H = 4224, CV_UH = 2304, CV_CH = CV_H + 2 * CV_UH;
constexpr int L_CV = 131072, L_RED = 102400, L_BN = 151552, L_MISC = 161792, L_BYTES = 163840;
enum { C_F2 = 0, C_F1, C_Y, C_GB, C_BAD = C_GB + 2, C_DONE = C_BAD + 2, C_GO, C_FO = C_GO + 4 };
static_assert(C_DONE - C_Y == 5, "attn_fast derives one address from the other");

static_assert(NPRE <= 28, "ring slot 3 is borrowed until the first barrier");
#define WVB(N) wait_vm_bar<N>()
#define UNR _Pragma("unroll 1")
#define TB_DUMP()
#define TS(slot)
#define TS_INIT()
#define TS_DUMP()

DEV unsigned cvtpk(float lo, float hi) { return __builtin_bit_cast(unsigned, __builtin_convertvector((f32x2){lo, hi}, bf16x2)); }
DEV float dot2(unsigned a, unsigned b, float c) { return __builtin_amdgcn_fdot2_f32_bf16(__builtin_bit_cast(bf16x2, a), __builtin_bit_cast(bf16x2, b), c, false); }
DEV float bflo(unsigned u) { return __uint_as_float(u << 16); }
DEV float bfhi(unsigned u) { return __uint_as_float(u & 0xffff0000u); }
DEV void setm0(unsigned v) { asm volatile("s_mov_b32 m0, %0\n\ts_nop 0" ::"s"(v) : "memory"); }
template <int OFF> DEV void glds(const void* sbase, unsigned voff) {
    asm volatile("global_load_lds_dwordx4 %0, %1 offset:%2" ::"v"(voff), "s"(sbase), "i"(OFF) : "memory");
}
template <int OFF> DEV void blds(u32x4 srd, unsigned voff, unsigned soff) {
    asm volatile("buffer_load_dwordx4 %0, %1, %2 offen offset:%3 lds" ::"v"(voff), "s"(srd), "s"(soff), "i"(OFF) : "memory");
}
DEV void gldsnt(const void* sbase, unsigned voff) { asm volatile("global_load_lds_dwordx4 %0, %1 nt" ::"v"(voff), "s"(sbase) : "memory"); }
#define LDNT(p) __builtin_nontemporal_load(p)
DEV s16x4 ldtr(lds_cptr p) { return __builtin_bit_cast(s16x4, __builtin_amdgcn_ds_read_tr16_b64_v4i16((__attribute__((address_space(3))) s16x4*)p)); }
DEV bf16x8 ldtr2(lds_cptr p0, lds_cptr p1) { s16x4 a = ldtr(p0), b = ldtr(p1); return (bf16x8){a[0], a[1], a[2], a[3], b[0], b[1], b[2], b[3]}; }
DEV bf16x8 ld128(lds_cptr p) { return *(const __attribute__((address_space(3))) bf16x8*)p; }
template <int N> DEV void wait_vm_bar() { asm volatile("s_waitcnt vmcnt(%0) lgkmcnt(0)\n\ts_barrier" ::"i"(N) : "memory"); }
DEV void wait_lgkm_bar() { asm volatile("s_waitcnt lgkmcnt(0)\n\ts_barrier" ::: "memory"); }

DEV void st16(rsrc_t r, unsigned off, u32x4 v) { __builtin_amdgcn_raw_buffer_store_b128(v, r, off, 0, 16); }
DEV void st8(rsrc_t r, unsigned off, u32x2 v) { __builtin_amdgcn_raw_buffer_store_b64(v, r, off, 0, 16); }
DEV void st4(rsrc_t r, unsigned off, unsigned v) { __builtin_amdgcn_raw_buffer_store_b32(v, r, off, 0, 16); }
DEV u32x4 ld16(rsrc_t r, unsigned off) { return __builtin_amdgcn_raw_buffer_load_b128(r, off, 0, 16); }
DEV u32x2 ld8(rsrc_t r, unsigned off) { return __builtin_amdgcn_raw_buffer_load_b64(r, off, 0, 16); }
DEV unsigned ld4(rsrc_t r, unsigned off) { return __builtin_amdgcn_raw_buffer_load_b32(r, off, 0, 16); }
DEV u64 mktag(u64 ep) {
    u64 e;
    asm volatile("s_waitcnt lgkmcnt(0)\n\ts_mov_b64 %0, %1" : "=s"(e) : "s"(ep));
    return e * 6364136223846793005ull + 1442695040888963407ull;
}
DEV unsigned arrive(lds_ptr lds, int c, int l) {
    asm volatile("s_waitcnt vmcnt(0) lgkmcnt(0)" ::: "memory");
    unsigned r = 0;
    if (l == 0) r = __hip_atomic_fetch_add((lds_u32)(lds + L_MISC) + c, 1u, RLX_WG);
    r = RFL(r);
    asm volatile("" ::: "memory");
    return r;
}
DEV void put_flag(char* ws, unsigned off, u64 tag, int l) { if (l == 0) __hip_atomic_store((gu64*)(ws + off), tag, RLX_AGENT); }
template <int SLP = 2> DEV void poll(char* ws, unsigned off, u64 tag) {
#pragma unroll 1
    for (int spin = 0; spin < (1 << 17); ++spin) {
        const u64 v = __hip_atomic_load((gu64*)(ws + off), RLX_AGENT);
        if (__all(v == tag)) break;
        __builtin_amdgcn_s_sleep(SLP);
    }
    __builtin_amdgcn_fence(__ATOMIC_ACQUIRE, "wavefront");
    asm volatile("" ::: "memory");
}
template <int SLP> DEV void lds_wait(lds_ptr lds, int c, unsigned target) {
#pragma unroll 1
    for (int spin = 0; spin < (1 << 20); ++spin) {
        const unsigned v = RFL(__hip_atomic_load((lds_u32)(lds + L_MISC) + c, RLX_WG));
        if (v >= target) break;
        __builtin_amdgcn_s_sleep(SLP);
    }
    asm volatile("" ::: "memory");
}
DEV void group_bar(lds_ptr lds, int X, int l, unsigned& gen) {
    asm volatile("s_waitcnt lgkmcnt(0)" ::: "memory");
    if (l == 0) __hip_atomic_fetch_add((lds_u32)(lds + L_MISC) + C_GB + X, 1u, RLX_WG);
    gen += 4;
    lds_wait<1>(lds, C_GB + X, gen);
}

constexpr int PR_W = 5120, PR_T = 4 * PR_W;
template <bool MAIN> DEV void prep_load(unsigned sl, const u16* bk, const u16* bv, const u16* shk, int l, int row, int ch) {
    const unsigned vo0 = MAIN ? (l >> 3) * 1280 + (l & 7) * 16 : ((l >> 3) + 8 * (l & 6)) * 1280 + (l & 1) * 16, vo1 = vo0 + 10240 - 1024;
    const char *kb = (const char*)bk + row * 1280 + ch * 2, *vb = (const char*)bv + row * 1280 + ch * 2;
    const char* hp = l < 32 ? (const char*)bk + ch * 2 + max(row * 1280 - 5120 + (int)vo0, 0) : (const char*)shk + ch * 8 + (l & (MAIN ? 31 : 7)) * 16;
    setm0(sl + 1024);
    if (MAIN)
        asm volatile("global_load_lds_dwordx4 %0, %2 nt\n\tglobal_load_lds_dwordx4 %1, %2 offset:1024 nt\n\t"
                     "global_load_lds_dwordx4 %0, %3 offset:2048 nt\n\tglobal_load_lds_dwordx4 %1, %3 offset:3072 nt"
                     ::"v"(vo0), "v"(vo1), "s"(kb), "s"(vb - 2048) : "memory");
    else
        asm volatile("global_load_lds_dwordx4 %0, %2\n\tglobal_load_lds_dwordx4 %1, %2 offset:1024\n\t"
                     "global_load_lds_dwordx4 %0, %3 offset:2048\n\tglobal_load_lds_dwordx4 %1, %3 offset:3072"
                     ::"v"(vo0), "v"(vo1), "s"(kb), "s"(vb - 2048) : "memory");
    setm0(sl);
    asm volatile("global_load_lds_dwordx4 %0, off" ::"v"(hp) : "memory");
}
DEV void prep_math(lds_cptr s, lds_ptr d, int l, bool first) {
    lds_cptr sp = s + ((l >> 2) & 3) * 128 + (l >> 4) * 32 + (l & 3) * 8;
    unsigned E[10], V[8];
    const u32x2 sh = *(const __attribute__((address_space(3))) u32x2*)(s + 512 + l * 8);
#pragma unroll
    for (int j = 0; j < 5; ++j) { const u32x2 a = __builtin_bit_cast(u32x2, ldtr(sp + (j ? 512 + 512 * j : 0))); E[2 * j] = a[0]; E[2 * j + 1] = a[1]; }
#pragma unroll
    for (int j = 0; j < 4; ++j) { const u32x2 a = __builtin_bit_cast(u32x2, ldtr(sp + 3072 + 512 * j)); V[2 * j] = a[0]; V[2 * j + 1] = a[1]; }
    if (first) E[0] = E[1] = 0;
    const unsigned s10 = __builtin_amdgcn_alignbit(sh[0], sh[0], 16), s32 = __builtin_amdgcn_alignbit(sh[1], sh[1], 16);
    unsigned pk[8], od = __builtin_amdgcn_alignbit(E[1], E[0], 16);
#pragma unroll
    for (int j = 0; j < 8; ++j) {
        const unsigned o2 = __builtin_amdgcn_alignbit(E[j + 2], E[j + 1], 16);
        const float a = dot2(o2, s10, dot2(od, s32, 0.f)), b = dot2(E[j + 2], s10, dot2(E[j + 1], s32, 0.f));
        pk[j] = cvtpk(a * bflo(V[j]), b * bfhi(V[j]));
        od = o2;
    }
    *(__attribute__((address_space(3))) u32x4*)d = (u32x4){pk[0], pk[1], pk[2], pk[3]};
    *(__attribute__((address_space(3))) u32x4*)(d + 16) = (u32x4){pk[4], pk[5], pk[6], pk[7]};
}
DEV unsigned prep_dst(int l, int tt, int c) {
    const int sl = c / 80;
    return OFF_U + sl * SS + (c - 80 * sl + (l >> 3)) * 4096 + tt * 128 + (l & 7) * 16;
}

template <bool V> DEV void gemm_loop(f32x16 (&a)[4], unsigned x0, unsigned x1, const unsigned (&wo)[4], int pub, const char* fa, u64 tag, unsigned ta, unsigned uo, const char* ws);
template <> DEV void gemm_loop<false>(f32x16 (&a)[4], unsigned x0, unsigned x1, const unsigned (&wo)[4], int pub, const char* fa, u64 tag, unsigned ta, unsigned uo, const char* ws) {
    unsigned it, dl, w0 = wo[0], w1 = wo[1], w2 = wo[2], w3 = wo[3];
    asm volatile(
        "v_mov_b64 v[32:33], 0\n\t"
        "v_mov_b64 v[34:35], 0\n\t"
        "v_mov_b64 v[36:37], 0\n\t"
        "v_mov_b64 v[38:39], 0\n\t"
        "v_mov_b64 v[40:41], 0\n\t"
        "v_mov_b64 v[42:43], 0\n\t"
        "v_mov_b64 v[44:45], 0\n\t"
        "v_mov_b64 v[46:47], 0\n\t"
        "v_mov_b64 v[48:49], 0\n\t"
        "v_mov_b64 v[50:51], 0\n\t"
        "v_mov_b64 v[52:53], 0\n\t"
        "v_mov_b64 v[54:55], 0\n\t"
        "v_mov_b64 v[56:57], 0\n\t"
        "v_mov_b64 v[58:59], 0\n\t"
        "v_mov_b64 v[60:61], 0\n\t"
        "v_mov_b64 v[62:63], 0\n\t"
        "v_mov_b64 v[64:65], 0\n\t"
        "v_mov_b64 v[66:67], 0\n\t"
        "v_mov_b64 v[68:69], 0\n\t"
        "v_mov_b64 v[70:71], 0\n\t"
        "v_mov_b64 v[72:73], 0\n\t"
        "v_mov_b64 v[74:75], 0\n\t"
        "v_mov_b64 v[76:77], 0\n\t"
        "v_mov_b64 v[78:79], 0\n\t"
        "v_mov_b64 v[80:81], 0\n\t"
        "v_mov_b64 v[82:83], 0\n\t"
        "v_mov_b64 v[84:85], 0\n\t"
        "v_mov_b64 v[86:87], 0\n\t"
        "v_mov_b64 v[88:89], 0\n\t"
        "v_mov_b64 v[90:91], 0\n\t"
        "v_mov_b64 v[92:93], 0\n\t"
        "v_mov_b64 v[94:95], 0\n\t"
        "s_mov_b32 %4, 0\n\t"
        "s_mov_b32 %5, 0x10000\n\t"
        "s_waitcnt lgkmcnt(0)\n\t"
        "s_barrier\n\t"
        "ds_read_b128 v[192:195], %15\n\t"
        "ds_read_b128 v[196:199], %15 offset:1152\n\t"
        "ds_read_b128 v[96:99], %6 offset:0\n\t"
        "ds_read_b64_tr_b16 v[112:113], %8 offset:0\n\t"
        "ds_read_b64_tr_b16 v[114:115], %8 offset:1024\n\t"
        "ds_read_b128 v[100:103], %6 offset:256\n\t"
        "ds_read_b128 v[104:107], %6 offset:512\n\t"
        "ds_read_b128 v[108:111], %6 offset:768\n\t"
        "ds_read_b64_tr_b16 v[116:117], %9 offset:0\n\t"
        "ds_read_b64_tr_b16 v[118:119], %9 offset:1024\n\t"
        "ds_read_b64_tr_b16 v[120:121], %10 offset:0\n\t"
        "ds_read_b64_tr_b16 v[122:123], %10 offset:1024\n\t"
        "ds_read_b64_tr_b16 v[124:125], %11 offset:0\n\t"
        "ds_read_b64_tr_b16 v[126:127], %11 offset:1024\n\t"
        "s_waitcnt lgkmcnt(12)\n\t"
        "global_store_dwordx4 %16, v[192:195], %17 sc1\n\t"
        "global_store_dwordx4 %18, v[196:199], %17 sc1\n\t"
        "ds_read_b128 v[128:131], %7 offset:0\n\t"
        "ds_read_b64_tr_b16 v[144:145], %8 offset:8192\n\t"
        "ds_read_b64_tr_b16 v[146:147], %8 offset:9216\n\t"
        "ds_read_b128 v[132:135], %7 offset:256\n\t"
        "ds_read_b128 v[136:139], %7 offset:512\n\t"
        "ds_read_b128 v[140:143], %7 offset:768\n\t"
        "ds_read_b64_tr_b16 v[148:149], %9 offset:8192\n\t"
        "ds_read_b64_tr_b16 v[150:151], %9 offset:9216\n\t"
        "ds_read_b64_tr_b16 v[152:153], %10 offset:8192\n\t"
        "ds_read_b64_tr_b16 v[154:155], %10 offset:9216\n\t"
        "ds_read_b64_tr_b16 v[156:157], %11 offset:8192\n\t"
        "ds_read_b64_tr_b16 v[158:159], %11 offset:9216\n\t"
        "LOOP%=:\n\t"
        "s_waitcnt lgkmcnt(12)\n\t"
        "v_mfma_f32_16x16x32_bf16 v[32:35], v[112:115], v[96:99], v[32:35]\n\t"
        "v_mfma_f32_16x16x32_bf16 v[48:51], v[112:115], v[100:103], v[48:51]\n\t"
        "v_mfma_f32_16x16x32_bf16 v[64:67], v[112:115], v[104:107], v[64:67]\n\t"
        "v_mfma_f32_16x16x32_bf16 v[80:83], v[112:115], v[108:111], v[80:83]\n\t"
        "v_mfma_f32_16x16x32_bf16 v[36:39], v[116:119], v[96:99], v[36:39]\n\t"
        "v_mfma_f32_16x16x32_bf16 v[52:55], v[116:119], v[100:103], v[52:55]\n\t"
        "v_mfma_f32_16x16x32_bf16 v[68:71], v[116:119], v[104:107], v[68:71]\n\t"
        "v_mfma_f32_16x16x32_bf16 v[84:87], v[116:119], v[108:111], v[84:87]\n\t"
        "v_mfma_f32_16x16x32_bf16 v[40:43], v[120:123], v[96:99], v[40:43]\n\t"
        "v_mfma_f32_16x16x32_bf16 v[56:59], v[120:123], v[100:103], v[56:59]\n\t"
        "v_mfma_f32_16x16x32_bf16 v[72:75], v[120:123], v[104:107], v[72:75]\n\t"
        "v_mfma_f32_16x16x32_bf16 v[88:91], v[120:123], v[108:111], v[88:91]\n\t"
        "v_mfma_f32_16x16x32_bf16 v[44:47], v[124:127], v[96:99], v[44:47]\n\t"
        "v_mfma_f32_16x16x32_bf16 v[60:63], v[124:127], v[100:103], v[60:63]\n\t"
        "v_mfma_f32_16x16x32_bf16 v[76:79], v[124:127], v[104:107], v[76:79]\n\t"
        "v_mfma_f32_16x16x32_bf16 v[92:95], v[124:127], v[108:111], v[92:95]\n\t"
        "s_cmp_lt_u32 %4, 1\n\t"
        "s_cbranch_scc1 NW%=\n\t"
        "s_waitcnt vmcnt(0)\n\t"
        "NW%=:\n\t"
        "s_waitcnt lgkmcnt(0)\n\t"
        "s_barrier\n\t"
        "s_cmp_eq_u32 %4, %12\n\t"
        "s_cbranch_scc0 NP%=\n\t"
        "s_mov_b64 exec, 1\n\t"
        "global_store_dwordx2 %13, %14, off sc1\n\t"
        "s_mov_b64 exec, -1\n\t"
        "NP%=:\n\t"
        "v_mfma_f32_16x16x32_bf16 v[32:35], v[144:147], v[128:131], v[32:35]\n\t"
        "ds_read_b128 v[96:99], %6 offset:32768\n\t"
        "ds_read_b64_tr_b16 v[112:113], %8 offset:32768\n\t"
        "v_mfma_f32_16x16x32_bf16 v[48:51], v[144:147], v[132:135], v[48:51]\n\t"
        "ds_read_b64_tr_b16 v[114:115], %8 offset:33792\n\t"
        "ds_read_b128 v[100:103], %6 offset:33024\n\t"
        "v_mfma_f32_16x16x32_bf16 v[64:67], v[144:147], v[136:139], v[64:67]\n\t"
        "ds_read_b128 v[104:107], %6 offset:33280\n\t"
        "ds_read_b128 v[108:111], %6 offset:33536\n\t"
        "v_mfma_f32_16x16x32_bf16 v[80:83], v[144:147], v[140:143], v[80:83]\n\t"
        "ds_read_b64_tr_b16 v[116:117], %9 offset:32768\n\t"
        "ds_read_b64_tr_b16 v[118:119], %9 offset:33792\n\t"
        "v_mfma_f32_16x16x32_bf16 v[36:39], v[148:151], v[128:131], v[36:39]\n\t"
        "ds_read_b64_tr_b16 v[120:121], %10 offset:32768\n\t"
        "ds_read_b64_tr_b16 v[122:123], %10 offset:33792\n\t"
        "v_mfma_f32_16x16x32_bf16 v[52:55], v[148:151], v[132:135], v[52:55]\n\t"
        "ds_read_b64_tr_b16 v[124:125], %11 offset:32768\n\t"
        "ds_read_b64_tr_b16 v[126:127], %11 offset:33792\n\t"
        "v_mfma_f32_16x16x32_bf16 v[68:71], v[148:151], v[136:139], v[68:71]\n\t"
        "ds_read_b128 v[160:163], %7 offset:32768\n\t"
        "ds_read_b64_tr_b16 v[176:177], %8 offset:40960\n\t"
        "v_mfma_f32_16x16x32_bf16 v[84:87], v[148:151], v[140:143], v[84:87]\n\t"
        "ds_read_b64_tr_b16 v[178:179], %8 offset:41984\n\t"
        "ds_read_b128 v[164:167], %7 offset:33024\n\t"
        "v_mfma_f32_16x16x32_bf16 v[40:43], v[152:155], v[128:131], v[40:43]\n\t"
        "ds_read_b128 v[168:171], %7 offset:33280\n\t"
        "v_mfma_f32_16x16x32_bf16 v[56:59], v[152:155], v[132:135], v[56:59]\n\t"
        "ds_read_b128 v[172:175], %7 offset:33536\n\t"
        "v_mfma_f32_16x16x32_bf16 v[72:75], v[152:155], v[136:139], v[72:75]\n\t"
        "ds_read_b64_tr_b16 v[180:181], %9 offset:40960\n\t"
        "v_mfma_f32_16x16x32_bf16 v[88:91], v[152:155], v[140:143], v[88:91]\n\t"
        "ds_read_b64_tr_b16 v[182:183], %9 offset:41984\n\t"
        "v_mfma_f32_16x16x32_bf16 v[44:47], v[156:159], v[128:131], v[44:47]\n\t"
        "ds_read_b64_tr_b16 v[184:185], %10 offset:40960\n\t"
        "v_mfma_f32_16x16x32_bf16 v[60:63], v[156:159], v[132:135], v[60:63]\n\t"
        "ds_read_b64_tr_b16 v[186:187], %10 offset:41984\n\t"
        "v_mfma_f32_16x16x32_bf16 v[76:79], v[156:159], v[136:139], v[76:79]\n\t"
        "ds_read_b64_tr_b16 v[188:189], %11 offset:40960\n\t"
        "v_mfma_f32_16x16x32_bf16 v[92:95], v[156:159], v[140:143], v[92:95]\n\t"
        "ds_read_b64_tr_b16 v[190:191], %11 offset:41984\n\t"
        "s_waitcnt lgkmcnt(12)\n\t"
        "v_mfma_f32_16x16x32_bf16 v[32:35], v[112:115], v[96:99], v[32:35]\n\t"
        "v_mfma_f32_16x16x32_bf16 v[48:51], v[112:115], v[100:103], v[48:51]\n\t"
        "v_mfma_f32_16x16x32_bf16 v[64:67], v[112:115], v[104:107], v[64:67]\n\t"
        "v_add_u32 %6, %5, %6\n\t"
        "v_mfma_f32_16x16x32_bf16 v[80:83], v[112:115], v[108:111], v[80:83]\n\t"
        "v_add_u32 %7, %5, %7\n\t"
        "v_mfma_f32_16x16x32_bf16 v[36:39], v[116:119], v[96:99], v[36:39]\n\t"
        "v_add_u32 %8, %5, %8\n\t"
        "v_mfma_f32_16x16x32_bf16 v[52:55], v[116:119], v[100:103], v[52:55]\n\t"
        "v_add_u32 %9, %5, %9\n\t"
        "v_mfma_f32_16x16x32_bf16 v[68:71], v[116:119], v[104:107], v[68:71]\n\t"
        "v_add_u32 %10, %5, %10\n\t"
        "v_mfma_f32_16x16x32_bf16 v[84:87], v[116:119], v[108:111], v[84:87]\n\t"
        "v_add_u32 %11, %5, %11\n\t"
        "v_mfma_f32_16x16x32_bf16 v[40:43], v[120:123], v[96:99], v[40:43]\n\t"
        "s_sub_u32 %5, 0, %5\n\t"
        "v_mfma_f32_16x16x32_bf16 v[56:59], v[120:123], v[100:103], v[56:59]\n\t"
        "v_mfma_f32_16x16x32_bf16 v[72:75], v[120:123], v[104:107], v[72:75]\n\t"
        "v_mfma_f32_16x16x32_bf16 v[88:91], v[120:123], v[108:111], v[88:91]\n\t"
        "v_mfma_f32_16x16x32_bf16 v[44:47], v[124:127], v[96:99], v[44:47]\n\t"
        "v_mfma_f32_16x16x32_bf16 v[60:63], v[124:127], v[100:103], v[60:63]\n\t"
        "v_mfma_f32_16x16x32_bf16 v[76:79], v[124:127], v[104:107], v[76:79]\n\t"
        "v_mfma_f32_16x16x32_bf16 v[92:95], v[124:127], v[108:111], v[92:95]\n\t"
        "s_cmp_eq_u32 %4, 4\n\t"
        "s_waitcnt lgkmcnt(0)\n\t"
        "s_cbranch_scc1 END%=\n\t"
        "s_barrier\n\t"
        "s_add_u32 %4, %4, 1\n\t"
        "v_mfma_f32_16x16x32_bf16 v[32:35], v[176:179], v[160:163], v[32:35]\n\t"
        "ds_read_b128 v[96:99], %6 offset:0\n\t"
        "ds_read_b64_tr_b16 v[112:113], %8 offset:0\n\t"
        "v_mfma_f32_16x16x32_bf16 v[48:51], v[176:179], v[164:167], v[48:51]\n\t"
        "ds_read_b64_tr_b16 v[114:115], %8 offset:1024\n\t"
        "ds_read_b128 v[100:103], %6 offset:256\n\t"
        "v_mfma_f32_16x16x32_bf16 v[64:67], v[176:179], v[168:171], v[64:67]\n\t"
        "ds_read_b128 v[104:107], %6 offset:512\n\t"
        "ds_read_b128 v[108:111], %6 offset:768\n\t"
        "v_mfma_f32_16x16x32_bf16 v[80:83], v[176:179], v[172:175], v[80:83]\n\t"
        "ds_read_b64_tr_b16 v[116:117], %9 offset:0\n\t"
        "ds_read_b64_tr_b16 v[118:119], %9 offset:1024\n\t"
        "v_mfma_f32_16x16x32_bf16 v[36:39], v[180:183], v[160:163], v[36:39]\n\t"
        "ds_read_b64_tr_b16 v[120:121], %10 offset:0\n\t"
        "ds_read_b64_tr_b16 v[122:123], %10 offset:1024\n\t"
        "v_mfma_f32_16x16x32_bf16 v[52:55], v[180:183], v[164:167], v[52:55]\n\t"
        "ds_read_b64_tr_b16 v[124:125], %11 offset:0\n\t"
        "ds_read_b64_tr_b16 v[126:127], %11 offset:1024\n\t"
        "v_mfma_f32_16x16x32_bf16 v[68:71], v[180:183], v[168:171], v[68:71]\n\t"
        "ds_read_b128 v[128:131], %7 offset:0\n\t"
        "ds_read_b64_tr_b16 v[144:145], %8 offset:8192\n\t"
        "v_mfma_f32_16x16x32_bf16 v[84:87], v[180:183], v[172:175], v[84:87]\n\t"
        "ds_read_b64_tr_b16 v[146:147], %8 offset:9216\n\t"
        "ds_read_b128 v[132:135], %7 offset:256\n\t"
        "v_mfma_f32_16x16x32_bf16 v[40:43], v[184:187], v[160:163], v[40:43]\n\t"
        "ds_read_b128 v[136:139], %7 offset:512\n\t"
        "v_mfma_f32_16x16x32_bf16 v[56:59], v[184:187], v[164:167], v[56:59]\n\t"
        "ds_read_b128 v[140:143], %7 offset:768\n\t"
        "v_mfma_f32_16x16x32_bf16 v[72:75], v[184:187], v[168:171], v[72:75]\n\t"
        "ds_read_b64_tr_b16 v[148:149], %9 offset:8192\n\t"
        "v_mfma_f32_16x16x32_bf16 v[88:91], v[184:187], v[172:175], v[88:91]\n\t"
        "ds_read_b64_tr_b16 v[150:151], %9 offset:9216\n\t"
        "v_mfma_f32_16x16x32_bf16 v[44:47], v[188:191], v[160:163], v[44:47]\n\t"
        "ds_read_b64_tr_b16 v[152:153], %10 offset:8192\n\t"
        "v_mfma_f32_16x16x32_bf16 v[60:63], v[188:191], v[164:167], v[60:63]\n\t"
        "ds_read_b64_tr_b16 v[154:155], %10 offset:9216\n\t"
        "v_mfma_f32_16x16x32_bf16 v[76:79], v[188:191], v[168:171], v[76:79]\n\t"
        "ds_read_b64_tr_b16 v[156:157], %11 offset:8192\n\t"
        "v_mfma_f32_16x16x32_bf16 v[92:95], v[188:191], v[172:175], v[92:95]\n\t"
        "ds_read_b64_tr_b16 v[158:159], %11 offset:9216\n\t"
        "s_branch LOOP%=\n\t"
        "END%=:\n\t"
        "v_mfma_f32_16x16x32_bf16 v[32:35], v[176:179], v[160:163], v[32:35]\n\t"
        "v_mfma_f32_16x16x32_bf16 v[48:51], v[176:179], v[164:167], v[48:51]\n\t"
        "v_mfma_f32_16x16x32_bf16 v[64:67], v[176:179], v[168:171], v[64:67]\n\t"
        "v_mfma_f32_16x16x32_bf16 v[80:83], v[176:179], v[172:175], v[80:83]\n\t"
        "v_mfma_f32_16x16x32_bf16 v[36:39], v[180:183], v[160:163], v[36:39]\n\t"
        "v_mfma_f32_16x16x32_bf16 v[52:55], v[180:183], v[164:167], v[52:55]\n\t"
        "v_mfma_f32_16x16x32_bf16 v[68:71], v[180:183], v[168:171], v[68:71]\n\t"
        "v_mfma_f32_16x16x32_bf16 v[84:87], v[180:183], v[172:175], v[84:87]\n\t"
        "v_mfma_f32_16x16x32_bf16 v[40:43], v[184:187], v[160:163], v[40:43]\n\t"
        "v_mfma_f32_16x16x32_bf16 v[56:59], v[184:187], v[164:167], v[56:59]\n\t"
        "v_mfma_f32_16x16x32_bf16 v[72:75], v[184:187], v[168:171], v[72:75]\n\t"
        "v_mfma_f32_16x16x32_bf16 v[88:91], v[184:187], v[172:175], v[88:91]\n\t"
        "v_mfma_f32_16x16x32_bf16 v[44:47], v[188:191], v[160:163], v[44:47]\n\t"
        "v_mfma_f32_16x16x32_bf16 v[60:63], v[188:191], v[164:167], v[60:63]\n\t"
        "v_mfma_f32_16x16x32_bf16 v[76:79], v[188:191], v[168:171], v[76:79]\n\t"
        "v_mfma_f32_16x16x32_bf16 v[92:95], v[188:191], v[172:175], v[92:95]\n\t"
        "s_nop 7\n\t"
        : "=&{v[32:47]}"(a[0]), "=&{v[48:63]}"(a[1]), "=&{v[64:79]}"(a[2]), "=&{v[80:95]}"(a[3]), "=&s"(it), "=&s"(dl),
          "+v"(x0), "+v"(x1), "+v"(w0), "+v"(w1), "+v"(w2), "+v"(w3)
        : "s"(pub), "v"(fa), "v"(tag), "v"(ta), "v"(uo), "s"(ws), "v"(uo + 32768)
        : "memory", "scc", "v96", "v97", "v98", "v99", "v100", "v101", "v102", "v103", "v104", "v105", "v106", "v107", "v108", "v109", "v110", "v111", "v112", "v113", "v114", "v115", "v116", "v117", "v118", "v119", "v120", "v121", "v122", "v123", "v124", "v125", "v126", "v127", "v128", "v129", "v130", "v131", "v132", "v133", "v134", "v135", "v136", "v137", "v138", "v139", "v140", "v141", "v142", "v143", "v144", "v145", "v146", "v147", "v148", "v149", "v150", "v151", "v152", "v153", "v154", "v155", "v156", "v157", "v158", "v159", "v160", "v161", "v162", "v163", "v164", "v165", "v166", "v167", "v168", "v169", "v170", "v171", "v172", "v173", "v174", "v175", "v176", "v177", "v178", "v179", "v180", "v181", "v182", "v183", "v184", "v185", "v186", "v187", "v188", "v189", "v190", "v191", "v192", "v193", "v194", "v195", "v196", "v197", "v198", "v199");
}
template <> DEV void gemm_loop<true>(f32x16 (&a)[4], unsigned x0, unsigned x1, const unsigned (&wo)[4], int pub, const char* fa, u64 tag, unsigned ta, unsigned uo, const char* ws) {
    unsigned it, dl, w0 = wo[0], w1 = wo[1], w2 = wo[2], w3 = wo[3];
    asm volatile(
        "v_mov_b64 v[32:33], 0\n\t"
        "v_mov_b64 v[34:35], 0\n\t"
        "v_mov_b64 v[36:37], 0\n\t"
        "v_mov_b64 v[38:39], 0\n\t"
        "v_mov_b64 v[40:41], 0\n\t"
        "v_mov_b64 v[42:43], 0\n\t"
        "v_mov_b64 v[44:45], 0\n\t"
        "v_mov_b64 v[46:47], 0\n\t"
        "v_mov_b64 v[48:49], 0\n\t"
        "v_mov_b64 v[50:51], 0\n\t"
        "v_mov_b64 v[52:53], 0\n\t"
        "v_mov_b64 v[54:55], 0\n\t"
        "v_mov_b64 v[56:57], 0\n\t"
        "v_mov_b64 v[58:59], 0\n\t"
        "v_mov_b64 v[60:61], 0\n\t"
        "v_mov_b64 v[62:63], 0\n\t"
        "v_mov_b64 v[64:65], 0\n\t"
        "v_mov_b64 v[66:67], 0\n\t"
        "v_mov_b64 v[68:69], 0\n\t"
        "v_mov_b64 v[70:71], 0\n\t"
        "v_mov_b64 v[72:73], 0\n\t"
        "v_mov_b64 v[74:75], 0\n\t"
        "v_mov_b64 v[76:77], 0\n\t"
        "v_mov_b64 v[78:79], 0\n\t"
        "v_mov_b64 v[80:81], 0\n\t"
        "v_mov_b64 v[82:83], 0\n\t"
        "v_mov_b64 v[84:85], 0\n\t"
        "v_mov_b64 v[86:87], 0\n\t"
        "v_mov_b64 v[88:89], 0\n\t"
        "v_mov_b64 v[90:91], 0\n\t"
        "v_mov_b64 v[92:93], 0\n\t"
        "v_mov_b64 v[94:95], 0\n\t"
        "s_mov_b32 %4, 0\n\t"
        "s_mov_b32 %5, 0x10000\n\t"
        "s_waitcnt lgkmcnt(0)\n\t"
        "s_barrier\n\t"
        "ds_read_b128 v[192:195], %15\n\t"
        "ds_read_b128 v[196:199], %15 offset:1152\n\t"
        "ds_read_b128 v[96:99], %6 offset:0\n\t"
        "ds_read_b64_tr_b16 v[112:113], %8 offset:0\n\t"
        "ds_read_b64_tr_b16 v[114:115], %8 offset:1024\n\t"
        "ds_read_b128 v[100:103], %6 offset:256\n\t"
        "ds_read_b128 v[104:107], %6 offset:512\n\t"
        "ds_read_b128 v[108:111], %6 offset:768\n\t"
        "ds_read_b64_tr_b16 v[116:117], %9 offset:0\n\t"
        "ds_read_b64_tr_b16 v[118:119], %9 offset:1024\n\t"
        "ds_read_b64_tr_b16 v[120:121], %10 offset:0\n\t"
        "ds_read_b64_tr_b16 v[122:123], %10 offset:1024\n\t"
        "ds_read_b64_tr_b16 v[124:125], %11 offset:0\n\t"
        "ds_read_b64_tr_b16 v[126:127], %11 offset:1024\n\t"
        "s_waitcnt lgkmcnt(12)\n\t"
        "global_store_dwordx4 %16, v[192:195], %17 sc1\n\t"
        "global_store_dwordx4 %18, v[196:199], %17 sc1\n\t"
        "ds_read_b128 v[128:131], %7 offset:0\n\t"
        "ds_read_b64_tr_b16 v[144:145], %8 offset:8192\n\t"
        "ds_read_b64_tr_b16 v[146:147], %8 offset:9216\n\t"
        "ds_read_b128 v[132:135], %7 offset:256\n\t"
        "ds_read_b128 v[136:139], %7 offset:512\n\t"
        "ds_read_b128 v[140:143], %7 offset:768\n\t"
        "ds_read_b64_tr_b16 v[148:149], %9 offset:8192\n\t"
        "ds_read_b64_tr_b16 v[150:151], %9 offset:9216\n\t"
        "ds_read_b64_tr_b16 v[152:153], %10 offset:8192\n\t"
        "ds_read_b64_tr_b16 v[154:155], %10 offset:9216\n\t"
        "ds_read_b64_tr_b16 v[156:157], %11 offset:8192\n\t"
        "ds_read_b64_tr_b16 v[158:159], %11 offset:9216\n\t"
        "LOOP%=:\n\t"
        "s_waitcnt lgkmcnt(12)\n\t"
        "v_mfma_f32_16x16x32_bf16 v[32:35], v[96:99], v[112:115], v[32:35]\n\t"
        "v_mfma_f32_16x16x32_bf16 v[48:51], v[100:103], v[112:115], v[48:51]\n\t"
        "v_mfma_f32_16x16x32_bf16 v[64:67], v[104:107], v[112:115], v[64:67]\n\t"
        "v_mfma_f32_16x16x32_bf16 v[80:83], v[108:111], v[112:115], v[80:83]\n\t"
        "v_mfma_f32_16x16x32_bf16 v[36:39], v[96:99], v[116:119], v[36:39]\n\t"
        "v_mfma_f32_16x16x32_bf16 v[52:55], v[100:103], v[116:119], v[52:55]\n\t"
        "v_mfma_f32_16x16x32_bf16 v[68:71], v[104:107], v[116:119], v[68:71]\n\t"
        "v_mfma_f32_16x16x32_bf16 v[84:87], v[108:111], v[116:119], v[84:87]\n\t"
        "v_mfma_f32_16x16x32_bf16 v[40:43], v[96:99], v[120:123], v[40:43]\n\t"
        "v_mfma_f32_16x16x32_bf16 v[56:59], v[100:103], v[120:123], v[56:59]\n\t"
        "v_mfma_f32_16x16x32_bf16 v[72:75], v[104:107], v[120:123], v[72:75]\n\t"
        "v_mfma_f32_16x16x32_bf16 v[88:91], v[108:111], v[120:123], v[88:91]\n\t"
        "v_mfma_f32_16x16x32_bf16 v[44:47], v[96:99], v[124:127], v[44:47]\n\t"
        "v_mfma_f32_16x16x32_bf16 v[60:63], v[100:103], v[124:127], v[60:63]\n\t"
        "v_mfma_f32_16x16x32_bf16 v[76:79], v[104:107], v[124:127], v[76:79]\n\t"
        "v_mfma_f32_16x16x32_bf16 v[92:95], v[108:111], v[124:127], v[92:95]\n\t"
        "s_cmp_lt_u32 %4, 1\n\t"
        "s_cbranch_scc1 NW%=\n\t"
        "s_waitcnt vmcnt(0)\n\t"
        "NW%=:\n\t"
        "s_waitcnt lgkmcnt(0)\n\t"
        "s_barrier\n\t"
        "s_cmp_eq_u32 %4, %12\n\t"
        "s_cbranch_scc0 NP%=\n\t"
        "s_mov_b64 exec, 1\n\t"
        "global_store_dwordx2 %13, %14, off sc1\n\t"
        "s_mov_b64 exec, -1\n\t"
        "NP%=:\n\t"
        "v_mfma_f32_16x16x32_bf16 v[32:35], v[128:131], v[144:147], v[32:35]\n\t"
        "ds_read_b128 v[96:99], %6 offset:32768\n\t"
        "ds_read_b64_tr_b16 v[112:113], %8 offset:32768\n\t"
        "v_mfma_f32_16x16x32_bf16 v[48:51], v[132:135], v[144:147], v[48:51]\n\t"
        "ds_read_b64_tr_b16 v[114:115], %8 offset:33792\n\t"
        "ds_read_b128 v[100:103], %6 offset:33024\n\t"
        "v_mfma_f32_16x16x32_bf16 v[64:67], v[136:139], v[144:147], v[64:67]\n\t"
        "ds_read_b128 v[104:107], %6 offset:33280\n\t"
        "ds_read_b128 v[108:111], %6 offset:33536\n\t"
        "v_mfma_f32_16x16x32_bf16 v[80:83], v[140:143], v[144:147], v[80:83]\n\t"
        "ds_read_b64_tr_b16 v[116:117], %9 offset:32768\n\t"
        "ds_read_b64_tr_b16 v[118:119], %9 offset:33792\n\t"
        "v_mfma_f32_16x16x32_bf16 v[36:39], v[128:131], v[148:151], v[36:39]\n\t"
        "ds_read_b64_tr_b16 v[120:121], %10 offset:32768\n\t"
        "ds_read_b64_tr_b16 v[122:123], %10 offset:33792\n\t"
        "v_mfma_f32_16x16x32_bf16 v[52:55], v[132:135], v[148:151], v[52:55]\n\t"
        "ds_read_b64_tr_b16 v[124:125], %11 offset:32768\n\t"
        "ds_read_b64_tr_b16 v[126:127], %11 offset:33792\n\t"
        "v_mfma_f32_16x16x32_bf16 v[68:71], v[136:139], v[148:151], v[68:71]\n\t"
        "ds_read_b128 v[160:163], %7 offset:32768\n\t"
        "ds_read_b64_tr_b16 v[176:177], %8 offset:40960\n\t"
        "v_mfma_f32_16x16x32_bf16 v[84:87], v[140:143], v[148:151], v[84:87]\n\t"
        "ds_read_b64_tr_b16 v[178:179], %8 offset:41984\n\t"
        "ds_read_b128 v[164:167], %7 offset:33024\n\t"
        "v_mfma_f32_16x16x32_bf16 v[40:43], v[128:131], v[152:155], v[40:43]\n\t"
        "ds_read_b128 v[168:171], %7 offset:33280\n\t"
        "v_mfma_f32_16x16x32_bf16 v[56:59], v[132:135], v[152:155], v[56:59]\n\t"
        "ds_read_b128 v[172:175], %7 offset:33536\n\t"
        "v_mfma_f32_16x16x32_bf16 v[72:75], v[136:139], v[152:155], v[72:75]\n\t"
        "ds_read_b64_tr_b16 v[180:181], %9 offset:40960\n\t"
        "v_mfma_f32_16x16x32_bf16 v[88:91], v[140:143], v[152:155], v[88:91]\n\t"
        "ds_read_b64_tr_b16 v[182:183], %9 offset:41984\n\t"
        "v_mfma_f32_16x16x32_bf16 v[44:47], v[128:131], v[156:159], v[44:47]\n\t"
        "ds_read_b64_tr_b16 v[184:185], %10 offset:40960\n\t"
        "v_mfma_f32_16x16x32_bf16 v[60:63], v[132:135], v[156:159], v[60:63]\n\t"
        "ds_read_b64_tr_b16 v[186:187], %10 offset:41984\n\t"
        "v_mfma_f32_16x16x32_bf16 v[76:79], v[136:139], v[156:159], v[76:79]\n\t"
        "ds_read_b64_tr_b16 v[188:189], %11 offset:40960\n\t"
        "v_mfma_f32_16x16x32_bf16 v[92:95], v[140:143], v[156:159], v[92:95]\n\t"
        "ds_read_b64_tr_b16 v[190:191], %11 offset:41984\n\t"
        "s_waitcnt lgkmcnt(12)\n\t"
        "v_mfma_f32_16x16x32_bf16 v[32:35], v[96:99], v[112:115], v[32:35]\n\t"
        "v_mfma_f32_16x16x32_bf16 v[48:51], v[100:103], v[112:115], v[48:51]\n\t"
        "v_mfma_f32_16x16x32_bf16 v[64:67], v[104:107], v[112:115], v[64:67]\n\t"
        "v_add_u32 %6, %5, %6\n\t"
        "v_mfma_f32_16x16x32_bf16 v[80:83], v[108:111], v[112:115], v[80:83]\n\t"
        "v_add_u32 %7, %5, %7\n\t"
        "v_mfma_f32_16x16x32_bf16 v[36:39], v[96:99], v[116:119], v[36:39]\n\t"
        "v_add_u32 %8, %5, %8\n\t"
        "v_mfma_f32_16x16x32_bf16 v[52:55], v[100:103], v[116:119], v[52:55]\n\t"
        "v_add_u32 %9, %5, %9\n\t"
        "v_mfma_f32_16x16x32_bf16 v[68:71], v[104:107], v[116:119], v[68:71]\n\t"
        "v_add_u32 %10, %5, %10\n\t"
        "v_mfma_f32_16x16x32_bf16 v[84:87], v[108:111], v[116:119], v[84:87]\n\t"
        "v_add_u32 %11, %5, %11\n\t"
        "v_mfma_f32_16x16x32_bf16 v[40:43], v[96:99], v[120:123], v[40:43]\n\t"
        "s_sub_u32 %5, 0, %5\n\t"
        "v_mfma_f32_16x16x32_bf16 v[56:59], v[100:103], v[120:123], v[56:59]\n\t"
        "v_mfma_f32_16x16x32_bf16 v[72:75], v[104:107], v[120:123], v[72:75]\n\t"
        "v_mfma_f32_16x16x32_bf16 v[88:91], v[108:111], v[120:123], v[88:91]\n\t"
        "v_mfma_f32_16x16x32_bf16 v[44:47], v[96:99], v[124:127], v[44:47]\n\t"
        "v_mfma_f32_16x16x32_bf16 v[60:63], v[100:103], v[124:127], v[60:63]\n\t"
        "v_mfma_f32_16x16x32_bf16 v[76:79], v[104:107], v[124:127], v[76:79]\n\t"
        "v_mfma_f32_16x16x32_bf16 v[92:95], v[108:111], v[124:127], v[92:95]\n\t"
        "s_cmp_eq_u32 %4, 4\n\t"
        "s_waitcnt lgkmcnt(0)\n\t"
        "s_cbranch_scc1 END%=\n\t"
        "s_barrier\n\t"
        "s_add_u32 %4, %4, 1\n\t"
        "v_mfma_f32_16x16x32_bf16 v[32:35], v[160:163], v[176:179], v[32:35]\n\t"
        "ds_read_b128 v[96:99], %6 offset:0\n\t"
        "ds_read_b64_tr_b16 v[112:113], %8 offset:0\n\t"
        "v_mfma_f32_16x16x32_bf16 v[48:51], v[164:167], v[176:179], v[48:51]\n\t"
        "ds_read_b64_tr_b16 v[114:115], %8 offset:1024\n\t"
        "ds_read_b128 v[100:103], %6 offset:256\n\t"
        "v_mfma_f32_16x16x32_bf16 v[64:67], v[168:171], v[176:179], v[64:67]\n\t"
        "ds_read_b128 v[104:107], %6 offset:512\n\t"
        "ds_read_b128 v[108:111], %6 offset:768\n\t"
        "v_mfma_f32_16x16x32_bf16 v[80:83], v[172:175], v[176:179], v[80:83]\n\t"
        "ds_read_b64_tr_b16 v[116:117], %9 offset:0\n\t"
        "ds_read_b64_tr_b16 v[118:119], %9 offset:1024\n\t"
        "v_mfma_f32_16x16x32_bf16 v[36:39], v[160:163], v[180:183], v[36:39]\n\t"
        "ds_read_b64_tr_b16 v[120:121], %10 offset:0\n\t"
        "ds_read_b64_tr_b16 v[122:123], %10 offset:1024\n\t"
        "v_mfma_f32_16x16x32_bf16 v[52:55], v[164:167], v[180:183], v[52:55]\n\t"
        "ds_read_b64_tr_b16 v[124:125], %11 offset:0\n\t"
        "ds_read_b64_tr_b16 v[126:127], %11 offset:1024\n\t"
        "v_mfma_f32_16x16x32_bf16 v[68:71], v[168:171], v[180:183], v[68:71]\n\t"
        "ds_read_b128 v[128:131], %7 offset:0\n\t"
        "ds_read_b64_tr_b16 v[144:145], %8 offset:8192\n\t"
        "v_mfma_f32_16x16x32_bf16 v[84:87], v[172:175], v[180:183], v[84:87]\n\t"
        "ds_read_b64_tr_b16 v[146:147], %8 offset:9216\n\t"
        "ds_read_b128 v[132:135], %7 offset:256\n\t"
        "v_mfma_f32_16x16x32_bf16 v[40:43], v[160:163], v[184:187], v[40:43]\n\t"
        "ds_read_b128 v[136:139], %7 offset:512\n\t"
        "v_mfma_f32_16x16x32_bf16 v[56:59], v[164:167], v[184:187], v[56:59]\n\t"
        "ds_read_b128 v[140:143], %7 offset:768\n\t"
        "v_mfma_f32_16x16x32_bf16 v[72:75], v[168:171], v[184:187], v[72:75]\n\t"
        "ds_read_b64_tr_b16 v[148:149], %9 offset:8192\n\t"
        "v_mfma_f32_16x16x32_bf16 v[88:91], v[172:175], v[184:187], v[88:91]\n\t"
        "ds_read_b64_tr_b16 v[150:151], %9 offset:9216\n\t"
        "v_mfma_f32_16x16x32_bf16 v[44:47], v[160:163], v[188:191], v[44:47]\n\t"
        "ds_read_b64_tr_b16 v[152:153], %10 offset:8192\n\t"
        "v_mfma_f32_16x16x32_bf16 v[60:63], v[164:167], v[188:191], v[60:63]\n\t"
        "ds_read_b64_tr_b16 v[154:155], %10 offset:9216\n\t"
        "v_mfma_f32_16x16x32_bf16 v[76:79], v[168:171], v[188:191], v[76:79]\n\t"
        "ds_read_b64_tr_b16 v[156:157], %11 offset:8192\n\t"
        "v_mfma_f32_16x16x32_bf16 v[92:95], v[172:175], v[188:191], v[92:95]\n\t"
        "ds_read_b64_tr_b16 v[158:159], %11 offset:9216\n\t"
        "s_branch LOOP%=\n\t"
        "END%=:\n\t"
        "v_mfma_f32_16x16x32_bf16 v[32:35], v[160:163], v[176:179], v[32:35]\n\t"
        "v_mfma_f32_16x16x32_bf16 v[48:51], v[164:167], v[176:179], v[48:51]\n\t"
        "v_mfma_f32_16x16x32_bf16 v[64:67], v[168:171], v[176:179], v[64:67]\n\t"
        "v_mfma_f32_16x16x32_bf16 v[80:83], v[172:175], v[176:179], v[80:83]\n\t"
        "v_mfma_f32_16x16x32_bf16 v[36:39], v[160:163], v[180:183], v[36:39]\n\t"
        "v_mfma_f32_16x16x32_bf16 v[52:55], v[164:167], v[180:183], v[52:55]\n\t"
        "v_mfma_f32_16x16x32_bf16 v[68:71], v[168:171], v[180:183], v[68:71]\n\t"
        "v_mfma_f32_16x16x32_bf16 v[84:87], v[172:175], v[180:183], v[84:87]\n\t"
        "v_mfma_f32_16x16x32_bf16 v[40:43], v[160:163], v[184:187], v[40:43]\n\t"
        "v_mfma_f32_16x16x32_bf16 v[56:59], v[164:167], v[184:187], v[56:59]\n\t"
        "v_mfma_f32_16x16x32_bf16 v[72:75], v[168:171], v[184:187], v[72:75]\n\t"
        "v_mfma_f32_16x16x32_bf16 v[88:91], v[172:175], v[184:187], v[88:91]\n\t"
        "v_mfma_f32_16x16x32_bf16 v[44:47], v[160:163], v[188:191], v[44:47]\n\t"
        "v_mfma_f32_16x16x32_bf16 v[60:63], v[164:167], v[188:191], v[60:63]\n\t"
        "v_mfma_f32_16x16x32_bf16 v[76:79], v[168:171], v[188:191], v[76:79]\n\t"
        "v_mfma_f32_16x16x32_bf16 v[92:95], v[172:175], v[188:191], v[92:95]\n\t"
        "s_nop 7\n\t"
        : "=&{v[32:47]}"(a[0]), "=&{v[48:63]}"(a[1]), "=&{v[64:79]}"(a[2]), "=&{v[80:95]}"(a[3]), "=&s"(it), "=&s"(dl),
          "+v"(x0), "+v"(x1), "+v"(w0), "+v"(w1), "+v"(w2), "+v"(w3)
        : "s"(pub), "v"(fa), "v"(tag), "v"(ta), "v"(uo), "s"(ws), "v"(uo + 32768)
        : "memory", "scc", "v96", "v97", "v98", "v99", "v100", "v101", "v102", "v103", "v104", "v105", "v106", "v107", "v108", "v109", "v110", "v111", "v112", "v113", "v114", "v115", "v116", "v117", "v118", "v119", "v120", "v121", "v122", "v123", "v124", "v125", "v126", "v127", "v128", "v129", "v130", "v131", "v132", "v133", "v134", "v135", "v136", "v137", "v138", "v139", "v140", "v141", "v142", "v143", "v144", "v145", "v146", "v147", "v148", "v149", "v150", "v151", "v152", "v153", "v154", "v155", "v156", "v157", "v158", "v159", "v160", "v161", "v162", "v163", "v164", "v165", "v166", "v167", "v168", "v169", "v170", "v171", "v172", "v173", "v174", "v175", "v176", "v177", "v178", "v179", "v180", "v181", "v182", "v183", "v184", "v185", "v186", "v187", "v188", "v189", "v190", "v191", "v192", "v193", "v194", "v195", "v196", "v197", "v198", "v199");
}

template <bool V>
DEV void gemm_a(lds_ptr lds, unsigned lds0, int l, int w, int gi, int gj, char* ws, rsrc_t rs, u64 tag, unsigned f2off, unsigned uo) {
    const int c16 = l & 15, G = l >> 4, q = c16 >> 2, pp = l & 3, th = w & 1, ch = w >> 1;
    const unsigned xrow = lds0 + (16 * (c16 & 7) + 8 * th + (c16 >> 3)) * 128;
    const int r3 = q | ((G & 1) << 2);
    unsigned wo[4];
#pragma unroll
    for (int T = 0; T < 4; ++T) wo[T] = lds0 + 16384 + (8 * G + q) * 256 + ((4 * ch + T) ^ r3) * 32 + pp * 8;
    const int jp = gj >= 10 ? gj - 10 : (gj >= 5 ? gj - 5 : gj);
    f32x16 av[4];
    gemm_loop<V>(av, xrow + ((G ^ (c16 & 7)) * 16), xrow + (((4 + G) ^ (c16 & 7)) * 16), wo, RFL(w == 0 ? (PUBB - 1) / 2 : -1), ws + f2off, tag,
                 lds0 + L_CV + PR_T + (16 * w + (l >> 3)) * 144 + (l & 7) * 16, uo, ws);
    f32x4 acc[4][4];
#pragma unroll
    for (int m = 0; m < 4; ++m)
#pragma unroll
        for (int T = 0; T < 4; ++T) acc[m][T] = (f32x4){av[m][4 * T], av[m][4 * T + 1], av[m][4 * T + 2], av[m][4 * T + 3]};
    TS(2);
    if (!V) {
        const bool isq = gj < 5;
        const float sc = isq ? QSCALE : 1.0f;
        const int ts = isq ? QT : KVT;
        const unsigned base = (isq ? OFF_Q : 0) + (4 * gi + 2 * th) * ts + c16 * 16;
#pragma unroll
        for (int u = 0; u < 2; ++u) {
            const int oct = 16 * jp + 8 * ch + 4 * u + 2 * (G & 1) + (G >> 1), p = oct / 5, o5 = oct - 5 * p;
            const unsigned off = base + p * PS + o5 * 512;
#pragma unroll
            for (int m = 0; m < 4; ++m) {
                const f32x4 a = acc[m][2 * u] * sc, b = acc[m][2 * u + 1] * sc;
                const auto s0 = __builtin_amdgcn_permlane16_swap(cvtpk(a[0], a[1]), cvtpk(b[0], b[1]), false, false);
                const auto s1 = __builtin_amdgcn_permlane16_swap(cvtpk(a[2], a[3]), cvtpk(b[2], b[3]), false, false);
                __builtin_amdgcn_raw_buffer_store_b128((u32x4){s0[0], s1[0], s0[1], s1[1]}, rs, off + (m >> 1) * ts + (m & 1) * 256, 0, QST);
            }
        }
    } else {
        const unsigned base = (4 * gi + 2 * th) * KVT + 2560 + G * 640;
#pragma unroll
        for (int T = 0; T < 4; ++T) {
            const int col = 128 * jp + 64 * ch + 16 * T + c16, p = col / 40, cc = col - 40 * p;
            const unsigned off = base + p * PS + cc * 16;
#pragma unroll
            for (int mm = 0; mm < 2; ++mm) {
                const f32x4 a = acc[2 * mm][T], b = acc[2 * mm + 1][T];
                __builtin_amdgcn_raw_buffer_store_b128((u32x4){cvtpk(a[0], a[1]), cvtpk(a[2], a[3]), cvtpk(b[0], b[1]), cvtpk(b[2], b[3])}, rs, off + mm * KVT, 0, QST);
            }
        }
    }
}

DEV void conv_asm(u32x4& pk, unsigned& z1, unsigned& z2, unsigned& z3, int cnt, unsigned ug, unsigned hf, bf16x8 fD) {
    asm volatile(
        "ds_read_b64_tr_b16 v[64:65], %5 offset:896\n\t"
        "ds_read_b64_tr_b16 v[66:67], %5 offset:3200\n\t"
        "ds_read_b64_tr_b16 v[80:81], %5 offset:640\n\t"
        "ds_read_b64_tr_b16 v[82:83], %5 offset:2944\n\t"
        "ds_read_b64_tr_b16 v[68:69], %5 offset:832\n\t"
        "ds_read_b64_tr_b16 v[70:71], %5 offset:3136\n\t"
        "ds_read_b64_tr_b16 v[72:73], %5 offset:768\n\t"
        "ds_read_b64_tr_b16 v[74:75], %5 offset:3072\n\t"
        "ds_read_b64_tr_b16 v[76:77], %5 offset:704\n\t"
        "ds_read_b64_tr_b16 v[78:79], %5 offset:3008\n\t"
        "ds_read_b64_tr_b16 v[96:97], %6 offset:32\n\t"
        "ds_read_b64_tr_b16 v[98:99], %6 offset:0\n\t"
        "ds_read_b64_tr_b16 v[84:85], %5 offset:576\n\t"
        "ds_read_b64_tr_b16 v[86:87], %5 offset:2880\n\t"
        "ds_read_b64_tr_b16 v[100:101], %6 offset:160\n\t"
        "ds_read_b64_tr_b16 v[102:103], %6 offset:128\n\t"
        "ds_read_b64_tr_b16 v[88:89], %5 offset:512\n\t"
        "ds_read_b64_tr_b16 v[90:91], %5 offset:2816\n\t"
        "ds_read_b64_tr_b16 v[104:105], %6 offset:288\n\t"
        "ds_read_b64_tr_b16 v[106:107], %6 offset:256\n\t"
        "s_waitcnt lgkmcnt(15)\n\t"
        "v_mfma_f32_32x32x16_bf16 v[48:63], v[64:67], %7, 0\n\t"
        "v_mfma_f32_32x32x16_bf16 v[32:47], v[80:83], %7, 0\n\t"
        "LOOP%=:\n\t"
        "s_waitcnt lgkmcnt(8)\n\t"
        "v_mfma_f32_32x32x16_bf16 v[48:63], v[64:67], v[96:99], v[48:63]\n\t"
        "s_cmp_eq_u32 %4, 0\n\t"
        "ds_read_b64_tr_b16 v[92:93], %5 offset:448\n\t"
        "ds_read_b64_tr_b16 v[94:95], %5 offset:2752\n\t"
        "v_mfma_f32_32x32x16_bf16 v[32:47], v[80:83], v[96:99], v[32:47]\n\t"
        "ds_read_b64_tr_b16 v[108:109], %6 offset:416\n\t"
        "ds_read_b64_tr_b16 v[110:111], %6 offset:384\n\t"
        "s_waitcnt lgkmcnt(8)\n\t"
        "v_mfma_f32_32x32x16_bf16 v[48:63], v[68:71], v[100:103], v[48:63]\n\t"
        "ds_read_b64_tr_b16 v[64:65], %5 offset:384\n\t"
        "ds_read_b64_tr_b16 v[66:67], %5 offset:2688\n\t"
        "v_mfma_f32_32x32x16_bf16 v[32:47], v[84:87], v[100:103], v[32:47]\n\t"
        "ds_read_b64_tr_b16 v[96:97], %6 offset:544\n\t"
        "ds_read_b64_tr_b16 v[98:99], %6 offset:512\n\t"
        "s_waitcnt lgkmcnt(8)\n\t"
        "v_mfma_f32_32x32x16_bf16 v[48:63], v[72:75], v[104:107], v[48:63]\n\t"
        "ds_read_b64_tr_b16 v[68:69], %5 offset:320\n\t"
        "ds_read_b64_tr_b16 v[70:71], %5 offset:2624\n\t"
        "v_mfma_f32_32x32x16_bf16 v[32:47], v[88:91], v[104:107], v[32:47]\n\t"
        "ds_read_b64_tr_b16 v[100:101], %6 offset:672\n\t"
        "ds_read_b64_tr_b16 v[102:103], %6 offset:640\n\t"
        "s_waitcnt lgkmcnt(8)\n\t"
        "v_mfma_f32_32x32x16_bf16 v[48:63], v[76:79], v[108:111], v[48:63]\n\t"
        "ds_read_b64_tr_b16 v[72:73], %5 offset:256\n\t"
        "ds_read_b64_tr_b16 v[74:75], %5 offset:2560\n\t"
        "v_mfma_f32_32x32x16_bf16 v[32:47], v[92:95], v[108:111], v[32:47]\n\t"
        "ds_read_b64_tr_b16 v[104:105], %6 offset:800\n\t"
        "ds_read_b64_tr_b16 v[106:107], %6 offset:768\n\t"
        "s_cbranch_scc1 TAIL%=\n\t"
        "s_waitcnt lgkmcnt(8)\n\t"
        "v_mfma_f32_32x32x16_bf16 v[48:63], v[80:83], v[96:99], v[48:63]\n\t"
        "s_sub_u32 %4, %4, 1\n\t"
        "ds_read_b64_tr_b16 v[76:77], %5 offset:192\n\t"
        "ds_read_b64_tr_b16 v[78:79], %5 offset:2496\n\t"
        "v_mfma_f32_32x32x16_bf16 v[32:47], v[64:67], v[96:99], v[32:47]\n\t"
        "ds_read_b64_tr_b16 v[108:109], %6 offset:928\n\t"
        "ds_read_b64_tr_b16 v[110:111], %6 offset:896\n\t"
        "s_waitcnt lgkmcnt(8)\n\t"
        "v_mfma_f32_32x32x16_bf16 v[48:63], v[84:87], v[100:103], v[48:63]\n\t"
        "ds_read_b64_tr_b16 v[80:81], %5 offset:128\n\t"
        "ds_read_b64_tr_b16 v[82:83], %5 offset:2432\n\t"
        "v_mfma_f32_32x32x16_bf16 v[32:47], v[68:71], v[100:103], v[32:47]\n\t"
        "ds_read_b64_tr_b16 v[96:97], %6 offset:1056\n\t"
        "ds_read_b64_tr_b16 v[98:99], %6 offset:1024\n\t"
        "s_waitcnt lgkmcnt(8)\n\t"
        "v_mfma_f32_32x32x16_bf16 v[48:63], v[88:91], v[104:107], v[48:63]\n\t"
        "ds_read_b64_tr_b16 v[84:85], %5 offset:64\n\t"
        "ds_read_b64_tr_b16 v[86:87], %5 offset:2368\n\t"
        "v_mfma_f32_32x32x16_bf16 v[32:47], v[72:75], v[104:107], v[32:47]\n\t"
        "ds_read_b64_tr_b16 v[100:101], %6 offset:1184\n\t"
        "ds_read_b64_tr_b16 v[102:103], %6 offset:1152\n\t"
        "s_waitcnt lgkmcnt(8)\n\t"
        "v_mfma_f32_32x32x16_bf16 v[48:63], v[92:95], v[108:111], v[48:63]\n\t"
        "ds_read_b64_tr_b16 v[88:89], %5 offset:0\n\t"
        "ds_read_b64_tr_b16 v[90:91], %5 offset:2304\n\t"
        "v_mfma_f32_32x32x16_bf16 v[32:47], v[76:79], v[108:111], v[32:47]\n\t"
        "ds_read_b64_tr_b16 v[104:105], %6 offset:1312\n\t"
        "ds_read_b64_tr_b16 v[106:107], %6 offset:1280\n\t"
        "v_add_u32 %5, 0xfffffe00, %5\n\t"
        "v_add_u32 %6, 0x400, %6\n\t"
        "s_branch LOOP%=\n\t"
        "TAIL%=:\n\t"
        "s_waitcnt lgkmcnt(8)\n\t"
        "v_mfma_f32_32x32x16_bf16 v[48:63], v[80:83], v[96:99], v[48:63]\n\t"
        "ds_read_b64_tr_b16 v[108:109], %6 offset:928\n\t"
        "ds_read_b64_tr_b16 v[110:111], %6 offset:896\n\t"
        "s_nop 4\n\t"
        "v_add_f32_dpp v112, v35, v34 wave_shr:1 row_mask:0xf bank_mask:0xf bound_ctrl:0\n\t"
        "s_waitcnt lgkmcnt(6)\n\t"
        "v_mfma_f32_32x32x16_bf16 v[48:63], v[84:87], v[100:103], v[48:63]\n\t"
        "v_add_f32_dpp v113, v35, v38 wave_ror:1 row_mask:0xf bank_mask:0xf\n\t"
        "v_add_f32_dpp v113, v39, v38 wave_shr:1 row_mask:0xf bank_mask:0xf\n\t"
        "v_add_f32_dpp v114, v39, v42 wave_ror:1 row_mask:0xf bank_mask:0xf\n\t"
        "v_add_f32_dpp v114, v43, v42 wave_shr:1 row_mask:0xf bank_mask:0xf\n\t"
        "v_add_f32_dpp v115, v43, v46 wave_ror:1 row_mask:0xf bank_mask:0xf\n\t"
        "v_add_f32_dpp v115, v47, v46 wave_shr:1 row_mask:0xf bank_mask:0xf\n\t"
        "s_waitcnt lgkmcnt(2)\n\t"
        "v_mfma_f32_32x32x16_bf16 v[48:63], v[88:91], v[104:107], v[48:63]\n\t"
        "v_add_f32_dpp v120, v112, v33 wave_shr:1 row_mask:0xf bank_mask:0xf bound_ctrl:0\n\t"
        "v_add_f32_dpp v121, v112, v37 wave_ror:1 row_mask:0xf bank_mask:0xf\n\t"
        "v_add_f32_dpp v121, v113, v37 wave_shr:1 row_mask:0xf bank_mask:0xf\n\t"
        "v_add_f32_dpp v122, v113, v41 wave_ror:1 row_mask:0xf bank_mask:0xf\n\t"
        "v_add_f32_dpp v122, v114, v41 wave_shr:1 row_mask:0xf bank_mask:0xf\n\t"
        "v_add_f32_dpp v123, v114, v45 wave_ror:1 row_mask:0xf bank_mask:0xf\n\t"
        "s_waitcnt lgkmcnt(0)\n\t"
        "v_mfma_f32_32x32x16_bf16 v[48:63], v[92:95], v[108:111], v[48:63]\n\t"
        "v_add_f32_dpp v123, v115, v45 wave_shr:1 row_mask:0xf bank_mask:0xf\n\t"
        "v_add_f32_dpp v128, v120, v32 wave_shr:1 row_mask:0xf bank_mask:0xf bound_ctrl:0\n\t"
        "v_add_f32_dpp v129, v120, v36 wave_ror:1 row_mask:0xf bank_mask:0xf\n\t"
        "v_add_f32_dpp v129, v121, v36 wave_shr:1 row_mask:0xf bank_mask:0xf\n\t"
        "v_add_f32_dpp v130, v121, v40 wave_ror:1 row_mask:0xf bank_mask:0xf\n\t"
        "v_add_f32_dpp v130, v122, v40 wave_shr:1 row_mask:0xf bank_mask:0xf\n\t"
        "v_add_f32_dpp v131, v122, v44 wave_ror:1 row_mask:0xf bank_mask:0xf\n\t"
        "v_add_f32_dpp v131, v123, v44 wave_shr:1 row_mask:0xf bank_mask:0xf\n\t"
        "v_cvt_pk_bf16_f32 v136, v128, v129\n\t"
        "v_cvt_pk_bf16_f32 v137, v130, v131\n\t"
        "s_nop 2\n\t"
        "v_add_f32_dpp v116, v47, v50 wave_ror:1 row_mask:0xf bank_mask:0xf\n\t"
        "v_add_f32_dpp v116, v51, v50 wave_shr:1 row_mask:0xf bank_mask:0xf\n\t"
        "v_add_f32_dpp v117, v51, v54 wave_ror:1 row_mask:0xf bank_mask:0xf\n\t"
        "v_add_f32_dpp v117, v55, v54 wave_shr:1 row_mask:0xf bank_mask:0xf\n\t"
        "v_add_f32_dpp v118, v55, v58 wave_ror:1 row_mask:0xf bank_mask:0xf\n\t"
        "v_add_f32_dpp v118, v59, v58 wave_shr:1 row_mask:0xf bank_mask:0xf\n\t"
        "v_add_f32_dpp v119, v59, v62 wave_ror:1 row_mask:0xf bank_mask:0xf\n\t"
        "v_add_f32_dpp v119, v63, v62 wave_shr:1 row_mask:0xf bank_mask:0xf\n\t"
        "v_add_f32_dpp v124, v115, v49 wave_ror:1 row_mask:0xf bank_mask:0xf\n\t"
        "v_add_f32_dpp v124, v116, v49 wave_shr:1 row_mask:0xf bank_mask:0xf\n\t"
        "v_add_f32_dpp v125, v116, v53 wave_ror:1 row_mask:0xf bank_mask:0xf\n\t"
        "v_add_f32_dpp v125, v117, v53 wave_shr:1 row_mask:0xf bank_mask:0xf\n\t"
        "v_add_f32_dpp v126, v117, v57 wave_ror:1 row_mask:0xf bank_mask:0xf\n\t"
        "v_add_f32_dpp v126, v118, v57 wave_shr:1 row_mask:0xf bank_mask:0xf\n\t"
        "v_add_f32_dpp v127, v118, v61 wave_ror:1 row_mask:0xf bank_mask:0xf\n\t"
        "v_add_f32_dpp v127, v119, v61 wave_shr:1 row_mask:0xf bank_mask:0xf\n\t"
        "v_add_f32_dpp v132, v123, v48 wave_ror:1 row_mask:0xf bank_mask:0xf\n\t"
        "v_add_f32_dpp v132, v124, v48 wave_shr:1 row_mask:0xf bank_mask:0xf\n\t"
        "v_add_f32_dpp v133, v124, v52 wave_ror:1 row_mask:0xf bank_mask:0xf\n\t"
        "v_add_f32_dpp v133, v125, v52 wave_shr:1 row_mask:0xf bank_mask:0xf\n\t"
        "v_add_f32_dpp v134, v125, v56 wave_ror:1 row_mask:0xf bank_mask:0xf\n\t"
        "v_add_f32_dpp v134, v126, v56 wave_shr:1 row_mask:0xf bank_mask:0xf\n\t"
        "v_add_f32_dpp v135, v126, v60 wave_ror:1 row_mask:0xf bank_mask:0xf\n\t"
        "v_add_f32_dpp v135, v127, v60 wave_shr:1 row_mask:0xf bank_mask:0xf\n\t"
        "v_cvt_pk_bf16_f32 v138, v132, v133\n\t"
        "v_cvt_pk_bf16_f32 v139, v134, v135\n\t"
        : "=&{v[136:139]}"(pk), "=&{v127}"(z1), "=&{v119}"(z2), "=&{v63}"(z3), "+s"(cnt), "+v"(ug), "+v"(hf)
        : "v"(fD)
        : "memory", "scc", "v32", "v33", "v34", "v35", "v36", "v37", "v38", "v39", "v40", "v41", "v42", "v43", "v44", "v45", "v46", "v47", "v48", "v49", "v50", "v51", "v52", "v53", "v54", "v55", "v56", "v57", "v58", "v59", "v60", "v61", "v62", "v64", "v65", "v66", "v67", "v68", "v69", "v70", "v71", "v72", "v73", "v74", "v75", "v76", "v77", "v78", "v79", "v80", "v81", "v82", "v83", "v84", "v85", "v86", "v87", "v88", "v89", "v90", "v91", "v92", "v93", "v94", "v95", "v96", "v97", "v98", "v99", "v100", "v101", "v102", "v103", "v104", "v105", "v106", "v107", "v108", "v109", "v110", "v111", "v112", "v113", "v114", "v115", "v116", "v117", "v118", "v120", "v121", "v122", "v123", "v124", "v125", "v126", "v128", "v129", "v130", "v131", "v132", "v133", "v134", "v135");
}

DEV void attn_slow(lds_ptr lds, int l, int X, int kq, rsrc_t rs, unsigned qb, unsigned kvb, unsigned& gen) {
    const int r32 = l & 31, hi = l >> 5;
    const unsigned o16 = X * 1536 + l * 16, o4 = 1024 + r32 * 16 + 8 * X, oV = 2560 + (hi * 40 + 20 * X + r32) * 16;
    lds_f mx = (lds_f)(lds + L_RED);
    for (int n = 0; n < 4; ++n) {
        const bf16x8 q16 = __builtin_bit_cast(bf16x8, ld16(rs, qb + n * QT + o16));
        s16x4 q4 = __builtin_bit_cast(s16x4, ld8(rs, qb + n * QT + o4));
        if (hi) q4 = (s16x4){0, 0, 0, 0};
        float m = -INFINITY;
        for (int kt = 0; kt < 16; ++kt) {
            const bf16x8 k16 = __builtin_bit_cast(bf16x8, ld16(rs, kvb + kt * KVT + o16));
            const s16x4 k4 = __builtin_bit_cast(s16x4, ld8(rs, kvb + kt * KVT + o4));
            f32x16 s = MFMA32(k16, q16, (f32x16){});
            s = MFMA32H(k4, q4, s);
#pragma unroll
            for (int r = 0; r < 16; ++r) m = fmaxf(m, s[r]);
        }
        m = fmaxf(m, __shfl_xor(m, 32));
        if (!hi) mx[kq * 128 + n * 32 + r32] = m;
    }
    group_bar(lds, X, l, gen);
    float mrow[4];
#pragma unroll
    for (int n = 0; n < 4; ++n) mrow[n] = fmaxf(fmaxf(mx[n * 32 + r32], mx[128 + n * 32 + r32]), fmaxf(mx[256 + n * 32 + r32], mx[384 + n * 32 + r32]));
    group_bar(lds, X, l, gen);
#pragma unroll
    for (int n = 0; n < 4; ++n) {
        const bf16x8 q16 = __builtin_bit_cast(bf16x8, ld16(rs, qb + n * QT + o16));
        s16x4 q4 = __builtin_bit_cast(s16x4, ld8(rs, qb + n * QT + o4));
        if (hi) q4 = (s16x4){0, 0, 0, 0};
        const float m = mrow[n];
        f32x16 o = {};
        for (int kt = 0; kt < 16; ++kt) {
            const bf16x8 k16 = __builtin_bit_cast(bf16x8, ld16(rs, kvb + kt * KVT + o16));
            const s16x4 k4 = __builtin_bit_cast(s16x4, ld8(rs, kvb + kt * KVT + o4));
            const unsigned one = r32 == 20 ? 0x3F803F80u : 0u;
            u32x4 v0 = {one, one, one, one}, v1 = v0;
            if (r32 < 20) { v0 = ld16(rs, kvb + kt * KVT + oV); v1 = ld16(rs, kvb + kt * KVT + oV + 1280); }
            f32x16 s = MFMA32(k16, q16, (f32x16){});
            s = MFMA32H(k4, q4, s);
            u32x4 pa, pb;
#pragma unroll
            for (int r = 0; r < 4; ++r) {
                pa[r] = cvtpk(__builtin_amdgcn_exp2f(s[2 * r] - m), __builtin_amdgcn_exp2f(s[2 * r + 1] - m));
                pb[r] = cvtpk(__builtin_amdgcn_exp2f(s[8 + 2 * r] - m), __builtin_amdgcn_exp2f(s[9 + 2 * r] - m));
            }
            o = MFMA32(__builtin_bit_cast(bf16x8, v0), __builtin_bit_cast(bf16x8, pa), o);
            o = MFMA32(__builtin_bit_cast(bf16x8, v1), __builtin_bit_cast(bf16x8, pb), o);
        }
#pragma unroll
        for (int rg = 0; rg < 3; ++rg)
            *(__attribute__((address_space(3))) f32x4*)(lds + L_RED + ((kq * 4 + n) * 3 + rg) * 1024 + l * 16) =
                (f32x4){o[4 * rg], o[4 * rg + 1], o[4 * rg + 2], o[4 * rg + 3]};
    }
}

DEV void attn_fast(unsigned ldsdst, unsigned ldsword, int l, int X, const char* qb, const char* kvb, const char* wob, unsigned wol, unsigned fo, const char* ws, u64 tag, unsigned gow, const char* tsp, unsigned tsl) {
    const uint64_t qa = (uint64_t)qb, ka = (uint64_t)kvb, wa = (uint64_t)wob;
    const u32x4 srdW = {(unsigned)RFL((unsigned)wa), (unsigned)RFL((unsigned)(wa >> 32)), 140u * 1280u, 0x20000u};
    const unsigned voW = (l / 10) * 1280 + (l % 10) * 16;
    const u32x4 srdK = {(unsigned)RFL((unsigned)ka), (unsigned)RFL((unsigned)(ka >> 32)), 16u * KVT, 0x20000u};
    const u32x4 srdQ = {(unsigned)RFL((unsigned)qa), (unsigned)RFL((unsigned)(qa >> 32)), 4u * QT, 0x20000u};
    const unsigned vo16 = X * 1536 + l * 16, vo4 = 1024 + (l & 31) * 16 + 8 * X, voV = 2560 + ((l >> 5) * 40 + 20 * X + (l & 31)) * 16, la = ldsdst + l * 16;
    const u64 ones = (l & 31) == 20 ? 0x3F803F803F803F80ull : 0ull;
    unsigned so, cnt, sw;
    asm volatile(
        "v_mov_b64 v[160:161], 0\n\t"
        "v_mov_b64 v[162:163], 0\n\t"
        "v_mov_b64 v[164:165], 0\n\t"
        "v_mov_b64 v[166:167], 0\n\t"
        "v_mov_b64 v[174:175], %13\n\t"
        "v_mov_b64 v[176:177], %13\n\t"
        "v_mov_b64 v[178:179], %13\n\t"
        "v_mov_b64 v[180:181], %13\n\t"
        "v_mov_b64 v[188:189], %13\n\t"
        "v_mov_b64 v[190:191], %13\n\t"
        "v_mov_b64 v[192:193], %13\n\t"
        "v_mov_b64 v[194:195], %13\n\t"
        "v_mov_b64 v[202:203], %13\n\t"
        "v_mov_b64 v[204:205], %13\n\t"
        "v_mov_b64 v[206:207], %13\n\t"
        "v_mov_b64 v[208:209], %13\n\t"
        "v_mov_b64 v[216:217], %13\n\t"
        "v_mov_b64 v[218:219], %13\n\t"
        "v_mov_b64 v[220:221], %13\n\t"
        "v_mov_b64 v[222:223], %13\n\t"
        "v_mov_b64 v[32:33], 0\n\t"
        "v_mov_b64 v[34:35], 0\n\t"
        "v_mov_b64 v[36:37], 0\n\t"
        "v_mov_b64 v[38:39], 0\n\t"
        "v_mov_b64 v[40:41], 0\n\t"
        "v_mov_b64 v[42:43], 0\n\t"
        "v_mov_b64 v[44:45], 0\n\t"
        "v_mov_b64 v[46:47], 0\n\t"
        "v_mov_b64 v[48:49], 0\n\t"
        "v_mov_b64 v[50:51], 0\n\t"
        "v_mov_b64 v[52:53], 0\n\t"
        "v_mov_b64 v[54:55], 0\n\t"
        "v_mov_b64 v[56:57], 0\n\t"
        "v_mov_b64 v[58:59], 0\n\t"
        "v_mov_b64 v[60:61], 0\n\t"
        "v_mov_b64 v[62:63], 0\n\t"
        "v_mov_b64 v[64:65], 0\n\t"
        "v_mov_b64 v[66:67], 0\n\t"
        "v_mov_b64 v[68:69], 0\n\t"
        "v_mov_b64 v[70:71], 0\n\t"
        "v_mov_b64 v[72:73], 0\n\t"
        "v_mov_b64 v[74:75], 0\n\t"
        "v_mov_b64 v[76:77], 0\n\t"
        "v_mov_b64 v[78:79], 0\n\t"
        "v_mov_b64 v[80:81], 0\n\t"
        "v_mov_b64 v[82:83], 0\n\t"
        "v_mov_b64 v[84:85], 0\n\t"
        "v_mov_b64 v[86:87], 0\n\t"
        "v_mov_b64 v[88:89], 0\n\t"
        "v_mov_b64 v[90:91], 0\n\t"
        "v_mov_b64 v[92:93], 0\n\t"
        "v_mov_b64 v[94:95], 0\n\t"
        "v_mov_b64 v[136:137], 0\n\t"
        "v_mov_b64 v[138:139], 0\n\t"
        "v_mov_b64 v[140:141], 0\n\t"
        "v_mov_b64 v[142:143], 0\n\t"
        "s_mov_b32 %2, 0\n\t"
        "s_mov_b32 m0, %16\n\t"
        "v_mov_b32 v112, 1\n\t"
        "s_mov_b32 %1, 0x20000\n\t"
        "s_mov_b64 vcc, 0\n\t"
        "s_cmp_lg_u32 %17, 0\n\t"
        "s_cbranch_scc1 BW%=\n\t"
        "global_load_dwordx2 v[96:97], %18, %19 sc1\n\t"
        "s_sleep 2\n\t"
        "global_load_dwordx2 v[98:99], %18, %19 sc1\n\t"
        "PL%=:\n\t"
        "s_waitcnt vmcnt(1)\n\t"
        "v_cmp_ne_u64 vcc, %20, v[96:97]\n\t"
        "s_and_b32 %0, vcc_lo, 0xffff000f\n\t"
        "s_cbranch_scc0 OK%=\n\t"
        "s_sleep 1\n\t"
        "global_load_dwordx2 v[96:97], %18, %19 sc1\n\t"
        "s_waitcnt vmcnt(1)\n\t"
        "v_cmp_ne_u64 vcc, %20, v[98:99]\n\t"
        "s_and_b32 %0, vcc_lo, 0xffff000f\n\t"
        "s_cbranch_scc0 OK%=\n\t"
        "s_sleep 1\n\t"
        "global_load_dwordx2 v[98:99], %18, %19 sc1\n\t"
        "s_sub_u32 %1, %1, 1\n\t"
        "s_cbranch_scc0 PL%=\n\t"
        "s_branch OK%=\n\t"
        "BW%=:\n\t"
        "ds_read_b32 v96, %21\n\t"
        "s_sub_u32 %1, %1, 1\n\t"
        "s_cbranch_scc1 OK%=\n\t"
        "s_waitcnt lgkmcnt(0)\n\t"
        "v_readfirstlane_b32 %0, v96\n\t"
        "s_cmp_lg_u32 %0, 0\n\t"
        "s_cbranch_scc1 OK%=\n\t"
        "s_sleep 4\n\t"
        "s_branch BW%=\n\t"
        "OK%=:\n\t"
        "s_mov_b32 %0, 0\n\t"
        "buffer_load_dwordx4 v[144:147], %3, %8, %0 offen sc1\n\t"
        "s_mov_b64 exec, %10\n\t"
        "buffer_load_dwordx2 v[160:161], %4, %8, %0 offen sc1\n\t"
        "s_mov_b64 exec, -1\n\t"
        "buffer_load_dwordx4 v[168:171], %3, %7, %0 offen sc1\n\t"
        "buffer_load_dwordx2 v[172:173], %4, %7, %0 offen sc1\n\t"
        "s_mov_b32 %0, 2560\n\t"
        "buffer_load_dwordx4 v[148:151], %3, %8, %0 offen sc1\n\t"
        "s_mov_b64 exec, %10\n\t"
        "buffer_load_dwordx2 v[162:163], %4, %8, %0 offen sc1\n\t"
        "s_mov_b64 exec, -1\n\t"
        "s_mov_b32 %0, 5120\n\t"
        "buffer_load_dwordx4 v[152:155], %3, %8, %0 offen sc1\n\t"
        "s_mov_b64 exec, %10\n\t"
        "buffer_load_dwordx2 v[164:165], %4, %8, %0 offen sc1\n\t"
        "s_mov_b64 exec, -1\n\t"
        "s_mov_b32 %0, 7680\n\t"
        "buffer_load_dwordx4 v[156:159], %3, %8, %0 offen sc1\n\t"
        "s_mov_b64 exec, %10\n\t"
        "buffer_load_dwordx2 v[166:167], %4, %8, %0 offen sc1\n\t"
        "s_mov_b64 exec, -1\n\t"
        "s_mov_b32 %0, 0\n\t"
        "s_mov_b64 exec, %9\n\t"
        "buffer_load_dwordx4 v[174:177], %5, %7, %0 offen sc1\n\t"
        "buffer_load_dwordx4 v[178:181], %5, %7, %0 offen offset:1280 sc1\n\t"
        "s_mov_b64 exec, -1\n\t"
        "s_mov_b32 %0, 5120\n\t"
        "buffer_load_dwordx4 v[182:185], %3, %7, %0 offen sc1\n\t"
        "buffer_load_dwordx2 v[186:187], %4, %7, %0 offen sc1\n\t"
        "s_mov_b64 exec, %9\n\t"
        "buffer_load_dwordx4 v[188:191], %5, %7, %0 offen sc1\n\t"
        "buffer_load_dwordx4 v[192:195], %5, %7, %0 offen offset:1280 sc1\n\t"
        "s_mov_b64 exec, -1\n\t"
        "s_mov_b32 %0, 10240\n\t"
        "buffer_load_dwordx4 v[196:199], %3, %7, %0 offen sc1\n\t"
        "buffer_load_dwordx2 v[200:201], %4, %7, %0 offen sc1\n\t"
        "s_mov_b64 exec, %9\n\t"
        "buffer_load_dwordx4 v[202:205], %5, %7, %0 offen sc1\n\t"
        "buffer_load_dwordx4 v[206:209], %5, %7, %0 offen offset:1280 sc1\n\t"
        "s_mov_b64 exec, -1\n\t"
        "s_cmp_eq_u32 vcc_lo, 0\n\t"
        "s_cbranch_scc1 OK2%=\n\t"
        "s_mov_b32 %1, 0x20000\n\t"
        "P2%=:\n\t"
        "global_load_dwordx2 v[114:115], %18, %19 sc1\n\t"
        "s_waitcnt vmcnt(0)\n\t"
        "v_cmp_ne_u64 vcc, %20, v[114:115]\n\t"
        "s_cmp_eq_u32 vcc_lo, 0\n\t"
        "s_cbranch_scc1 OK2%=\n\t"
        "s_sleep 1\n\t"
        "s_sub_u32 %1, %1, 1\n\t"
        "s_cbranch_scc0 P2%=\n\t"
        "OK2%=:\n\t"
        "ds_write_b32 %21, v112\n\t"
        "s_mov_b32 %0, 15360\n\t"
        "s_mov_b32 %1, 4\n\t"
        "s_waitcnt vmcnt(16)\n\t"
        "v_mfma_f32_32x32x16_bf16 v[96:111], v[168:171], v[144:147], 0\n\t"
        "s_nop 1\n\t"
        "v_mfma_f32_32x32x8_bf16 v[96:111], v[172:173], v[160:161], v[96:111]\n\t"
        "s_nop 7\n\t"
        "s_nop 3\n\t"
        "s_waitcnt vmcnt(8) lgkmcnt(0)\n\t"
        "LOOP%=:\n\t"
        "v_mfma_f32_32x32x16_bf16 v[112:127], v[168:171], v[148:151], 0\n\t"
        "buffer_load_dwordx4 v[210:213], %3, %7, %0 offen sc1\n\t"
        "v_exp_f32 v96, v96\n\t"
        "v_exp_f32 v97, v97\n\t"
        "v_exp_f32 v98, v98\n\t"
        "v_exp_f32 v99, v99\n\t"
        "v_exp_f32 v100, v100\n\t"
        "v_mfma_f32_32x32x16_bf16 v[80:95], v[216:219], v[136:139], v[80:95]\n\t"
        "v_exp_f32 v101, v101\n\t"
        "v_exp_f32 v102, v102\n\t"
        "v_exp_f32 v103, v103\n\t"
        "v_exp_f32 v104, v104\n\t"
        "v_exp_f32 v105, v105\n\t"
        "v_mfma_f32_32x32x8_bf16 v[112:127], v[172:173], v[162:163], v[112:127]\n\t"
        "v_exp_f32 v106, v106\n\t"
        "v_exp_f32 v107, v107\n\t"
        "v_exp_f32 v108, v108\n\t"
        "v_exp_f32 v109, v109\n\t"
        "v_exp_f32 v110, v110\n\t"
        "v_exp_f32 v111, v111\n\t"
        "v_mfma_f32_32x32x16_bf16 v[80:95], v[220:223], v[140:143], v[80:95]\n\t"
        "v_cvt_pk_bf16_f32 v128, v96, v97\n\t"
        "v_cvt_pk_bf16_f32 v129, v98, v99\n\t"
        "v_cvt_pk_bf16_f32 v130, v100, v101\n\t"
        "v_cvt_pk_bf16_f32 v131, v102, v103\n\t"
        "v_cvt_pk_bf16_f32 v132, v104, v105\n\t"
        "v_cvt_pk_bf16_f32 v133, v106, v107\n\t"
        "v_cvt_pk_bf16_f32 v134, v108, v109\n\t"
        "v_cvt_pk_bf16_f32 v135, v110, v111\n\t"
        "v_mfma_f32_32x32x16_bf16 v[96:111], v[168:171], v[152:155], 0\n\t"
        "buffer_load_dwordx2 v[214:215], %4, %7, %0 offen sc1\n\t"
        "s_cmp_eq_u32 %1, 2\n\t"
        "s_cbranch_scc0 NPR%=\n\t"
        "s_setprio 0\n\t"
        "NPR%=:\n\t"
        "v_exp_f32 v112, v112\n\t"
        "v_exp_f32 v113, v113\n\t"
        "v_exp_f32 v114, v114\n\t"
        "v_exp_f32 v115, v115\n\t"
        "v_exp_f32 v116, v116\n\t"
        "v_mfma_f32_32x32x16_bf16 v[32:47], v[174:177], v[128:131], v[32:47]\n\t"
        "v_exp_f32 v117, v117\n\t"
        "v_exp_f32 v118, v118\n\t"
        "v_exp_f32 v119, v119\n\t"
        "v_exp_f32 v120, v120\n\t"
        "v_exp_f32 v121, v121\n\t"
        "v_mfma_f32_32x32x8_bf16 v[96:111], v[172:173], v[164:165], v[96:111]\n\t"
        "v_exp_f32 v122, v122\n\t"
        "v_exp_f32 v123, v123\n\t"
        "v_exp_f32 v124, v124\n\t"
        "v_exp_f32 v125, v125\n\t"
        "v_exp_f32 v126, v126\n\t"
        "v_exp_f32 v127, v127\n\t"
        "v_mfma_f32_32x32x16_bf16 v[32:47], v[178:181], v[132:135], v[32:47]\n\t"
        "v_cvt_pk_bf16_f32 v136, v112, v113\n\t"
        "v_cvt_pk_bf16_f32 v137, v114, v115\n\t"
        "v_cvt_pk_bf16_f32 v138, v116, v117\n\t"
        "v_cvt_pk_bf16_f32 v139, v118, v119\n\t"
        "v_cvt_pk_bf16_f32 v140, v120, v121\n\t"
        "v_cvt_pk_bf16_f32 v141, v122, v123\n\t"
        "v_cvt_pk_bf16_f32 v142, v124, v125\n\t"
        "v_cvt_pk_bf16_f32 v143, v126, v127\n\t"
        "v_mfma_f32_32x32x16_bf16 v[112:127], v[168:171], v[156:159], 0\n\t"
        "s_mov_b64 exec, %9\n\t"
        "buffer_load_dwordx4 v[216:219], %5, %7, %0 offen sc1\n\t"
        "buffer_load_dwordx4 v[220:223], %5, %7, %0 offen offset:1280 sc1\n\t"
        "s_mov_b64 exec, %15\n\t"
        "buffer_load_dwordx4 %12, %14, %2 offen lds\n\t"
        "s_mov_b64 exec, -1\n\t"
        "v_exp_f32 v96, v96\n\t"
        "v_exp_f32 v97, v97\n\t"
        "v_exp_f32 v98, v98\n\t"
        "v_exp_f32 v99, v99\n\t"
        "v_exp_f32 v100, v100\n\t"
        "v_mfma_f32_32x32x16_bf16 v[48:63], v[174:177], v[136:139], v[48:63]\n\t"
        "v_exp_f32 v101, v101\n\t"
        "v_exp_f32 v102, v102\n\t"
        "v_exp_f32 v103, v103\n\t"
        "v_exp_f32 v104, v104\n\t"
        "v_exp_f32 v105, v105\n\t"
        "v_mfma_f32_32x32x8_bf16 v[112:127], v[172:173], v[166:167], v[112:127]\n\t"
        "v_exp_f32 v106, v106\n\t"
        "v_exp_f32 v107, v107\n\t"
        "v_exp_f32 v108, v108\n\t"
        "v_exp_f32 v109, v109\n\t"
        "v_exp_f32 v110, v110\n\t"
        "v_exp_f32 v111, v111\n\t"
        "v_mfma_f32_32x32x16_bf16 v[48:63], v[178:181], v[140:143], v[48:63]\n\t"
        "v_cvt_pk_bf16_f32 v128, v96, v97\n\t"
        "v_cvt_pk_bf16_f32 v129, v98, v99\n\t"
        "v_cvt_pk_bf16_f32 v130, v100, v101\n\t"
        "v_cvt_pk_bf16_f32 v131, v102, v103\n\t"
        "v_cvt_pk_bf16_f32 v132, v104, v105\n\t"
        "v_cvt_pk_bf16_f32 v133, v106, v107\n\t"
        "v_cvt_pk_bf16_f32 v134, v108, v109\n\t"
        "v_cvt_pk_bf16_f32 v135, v110, v111\n\t"
        "s_waitcnt vmcnt(9)\n\t"
        "v_mfma_f32_32x32x16_bf16 v[96:111], v[182:185], v[144:147], 0\n\t"
        "s_add_u32 %0, %0, 5120\n\t"
        "s_add_u32 m0, m0, 800\n\t"
        "s_add_u32 %2, %2, 6400\n\t"
        "v_exp_f32 v112, v112\n\t"
        "v_exp_f32 v113, v113\n\t"
        "v_exp_f32 v114, v114\n\t"
        "v_exp_f32 v115, v115\n\t"
        "v_exp_f32 v116, v116\n\t"
        "v_mfma_f32_32x32x16_bf16 v[64:79], v[174:177], v[128:131], v[64:79]\n\t"
        "v_exp_f32 v117, v117\n\t"
        "v_exp_f32 v118, v118\n\t"
        "v_exp_f32 v119, v119\n\t"
        "v_exp_f32 v120, v120\n\t"
        "v_exp_f32 v121, v121\n\t"
        "v_mfma_f32_32x32x8_bf16 v[96:111], v[186:187], v[160:161], v[96:111]\n\t"
        "v_exp_f32 v122, v122\n\t"
        "v_exp_f32 v123, v123\n\t"
        "v_exp_f32 v124, v124\n\t"
        "v_exp_f32 v125, v125\n\t"
        "v_exp_f32 v126, v126\n\t"
        "v_exp_f32 v127, v127\n\t"
        "v_mfma_f32_32x32x16_bf16 v[64:79], v[178:181], v[132:135], v[64:79]\n\t"
        "v_cvt_pk_bf16_f32 v136, v112, v113\n\t"
        "v_cvt_pk_bf16_f32 v137, v114, v115\n\t"
        "v_cvt_pk_bf16_f32 v138, v116, v117\n\t"
        "v_cvt_pk_bf16_f32 v139, v118, v119\n\t"
        "v_cvt_pk_bf16_f32 v140, v120, v121\n\t"
        "v_cvt_pk_bf16_f32 v141, v122, v123\n\t"
        "v_cvt_pk_bf16_f32 v142, v124, v125\n\t"
        "v_cvt_pk_bf16_f32 v143, v126, v127\n\t"
        "v_mfma_f32_32x32x16_bf16 v[112:127], v[182:185], v[148:151], 0\n\t"
        "buffer_load_dwordx4 v[168:171], %3, %7, %0 offen sc1\n\t"
        "v_exp_f32 v96, v96\n\t"
        "v_exp_f32 v97, v97\n\t"
        "v_exp_f32 v98, v98\n\t"
        "v_exp_f32 v99, v99\n\t"
        "v_exp_f32 v100, v100\n\t"
        "v_mfma_f32_32x32x16_bf16 v[80:95], v[174:177], v[136:139], v[80:95]\n\t"
        "v_exp_f32 v101, v101\n\t"
        "v_exp_f32 v102, v102\n\t"
        "v_exp_f32 v103, v103\n\t"
        "v_exp_f32 v104, v104\n\t"
        "v_exp_f32 v105, v105\n\t"
        "v_mfma_f32_32x32x8_bf16 v[112:127], v[186:187], v[162:163], v[112:127]\n\t"
        "v_exp_f32 v106, v106\n\t"
        "v_exp_f32 v107, v107\n\t"
        "v_exp_f32 v108, v108\n\t"
        "v_exp_f32 v109, v109\n\t"
        "v_exp_f32 v110, v110\n\t"
        "v_exp_f32 v111, v111\n\t"
        "v_mfma_f32_32x32x16_bf16 v[80:95], v[178:181], v[140:143], v[80:95]\n\t"
        "v_cvt_pk_bf16_f32 v128, v96, v97\n\t"
        "v_cvt_pk_bf16_f32 v129, v98, v99\n\t"
        "v_cvt_pk_bf16_f32 v130, v100, v101\n\t"
        "v_cvt_pk_bf16_f32 v131, v102, v103\n\t"
        "v_cvt_pk_bf16_f32 v132, v104, v105\n\t"
        "v_cvt_pk_bf16_f32 v133, v106, v107\n\t"
        "v_cvt_pk_bf16_f32 v134, v108, v109\n\t"
        "v_cvt_pk_bf16_f32 v135, v110, v111\n\t"
        "v_mfma_f32_32x32x16_bf16 v[96:111], v[182:185], v[152:155], 0\n\t"
        "buffer_load_dwordx2 v[172:173], %4, %7, %0 offen sc1\n\t"
        "v_exp_f32 v112, v112\n\t"
        "v_exp_f32 v113, v113\n\t"
        "v_exp_f32 v114, v114\n\t"
        "v_exp_f32 v115, v115\n\t"
        "v_exp_f32 v116, v116\n\t"
        "v_mfma_f32_32x32x16_bf16 v[32:47], v[188:191], v[128:131], v[32:47]\n\t"
        "v_exp_f32 v117, v117\n\t"
        "v_exp_f32 v118, v118\n\t"
        "v_exp_f32 v119, v119\n\t"
        "v_exp_f32 v120, v120\n\t"
        "v_exp_f32 v121, v121\n\t"
        "v_mfma_f32_32x32x8_bf16 v[96:111], v[186:187], v[164:165], v[96:111]\n\t"
        "v_exp_f32 v122, v122\n\t"
        "v_exp_f32 v123, v123\n\t"
        "v_exp_f32 v124, v124\n\t"
        "v_exp_f32 v125, v125\n\t"
        "v_exp_f32 v126, v126\n\t"
        "v_exp_f32 v127, v127\n\t"
        "v_mfma_f32_32x32x16_bf16 v[32:47], v[192:195], v[132:135], v[32:47]\n\t"
        "v_cvt_pk_bf16_f32 v136, v112, v113\n\t"
        "v_cvt_pk_bf16_f32 v137, v114, v115\n\t"
        "v_cvt_pk_bf16_f32 v138, v116, v117\n\t"
        "v_cvt_pk_bf16_f32 v139, v118, v119\n\t"
        "v_cvt_pk_bf16_f32 v140, v120, v121\n\t"
        "v_cvt_pk_bf16_f32 v141, v122, v123\n\t"
        "v_cvt_pk_bf16_f32 v142, v124, v125\n\t"
        "v_cvt_pk_bf16_f32 v143, v126, v127\n\t"
        "v_mfma_f32_32x32x16_bf16 v[112:127], v[182:185], v[156:159], 0\n\t"
        "s_mov_b64 exec, %9\n\t"
        "buffer_load_dwordx4 v[174:177], %5, %7, %0 offen sc1\n\t"
        "buffer_load_dwordx4 v[178:181], %5, %7, %0 offen offset:1280 sc1\n\t"
        "s_mov_b64 exec, %15\n\t"
        "buffer_load_dwordx4 %12, %14, %2 offen lds\n\t"
        "s_mov_b64 exec, -1\n\t"
        "v_exp_f32 v96, v96\n\t"
        "v_exp_f32 v97, v97\n\t"
        "v_exp_f32 v98, v98\n\t"
        "v_exp_f32 v99, v99\n\t"
        "v_exp_f32 v100, v100\n\t"
        "v_mfma_f32_32x32x16_bf16 v[48:63], v[188:191], v[136:139], v[48:63]\n\t"
        "v_exp_f32 v101, v101\n\t"
        "v_exp_f32 v102, v102\n\t"
        "v_exp_f32 v103, v103\n\t"
        "v_exp_f32 v104, v104\n\t"
        "v_exp_f32 v105, v105\n\t"
        "v_mfma_f32_32x32x8_bf16 v[112:127], v[186:187], v[166:167], v[112:127]\n\t"
        "v_exp_f32 v106, v106\n\t"
        "v_exp_f32 v107, v107\n\t"
        "v_exp_f32 v108, v108\n\t"
        "v_exp_f32 v109, v109\n\t"
        "v_exp_f32 v110, v110\n\t"
        "v_exp_f32 v111, v111\n\t"
        "v_mfma_f32_32x32x16_bf16 v[48:63], v[192:195], v[140:143], v[48:63]\n\t"
        "v_cvt_pk_bf16_f32 v128, v96, v97\n\t"
        "v_cvt_pk_bf16_f32 v129, v98, v99\n\t"
        "v_cvt_pk_bf16_f32 v130, v100, v101\n\t"
        "v_cvt_pk_bf16_f32 v131, v102, v103\n\t"
        "v_cvt_pk_bf16_f32 v132, v104, v105\n\t"
        "v_cvt_pk_bf16_f32 v133, v106, v107\n\t"
        "v_cvt_pk_bf16_f32 v134, v108, v109\n\t"
        "v_cvt_pk_bf16_f32 v135, v110, v111\n\t"
        "s_waitcnt vmcnt(10)\n\t"
        "v_mfma_f32_32x32x16_bf16 v[96:111], v[196:199], v[144:147], 0\n\t"
        "s_add_u32 %0, %0, 5120\n\t"
        "s_add_u32 m0, m0, 800\n\t"
        "s_add_u32 %2, %2, 6400\n\t"
        "v_exp_f32 v112, v112\n\t"
        "v_exp_f32 v113, v113\n\t"
        "v_exp_f32 v114, v114\n\t"
        "v_exp_f32 v115, v115\n\t"
        "v_exp_f32 v116, v116\n\t"
        "v_mfma_f32_32x32x16_bf16 v[64:79], v[188:191], v[128:131], v[64:79]\n\t"
        "v_exp_f32 v117, v117\n\t"
        "v_exp_f32 v118, v118\n\t"
        "v_exp_f32 v119, v119\n\t"
        "v_exp_f32 v120, v120\n\t"
        "v_exp_f32 v121, v121\n\t"
        "v_mfma_f32_32x32x8_bf16 v[96:111], v[200:201], v[160:161], v[96:111]\n\t"
        "v_exp_f32 v122, v122\n\t"
        "v_exp_f32 v123, v123\n\t"
        "v_exp_f32 v124, v124\n\t"
        "v_exp_f32 v125, v125\n\t"
        "v_exp_f32 v126, v126\n\t"
        "v_exp_f32 v127, v127\n\t"
        "v_mfma_f32_32x32x16_bf16 v[64:79], v[192:195], v[132:135], v[64:79]\n\t"
        "v_cvt_pk_bf16_f32 v136, v112, v113\n\t"
        "v_cvt_pk_bf16_f32 v137, v114, v115\n\t"
        "v_cvt_pk_bf16_f32 v138, v116, v117\n\t"
        "v_cvt_pk_bf16_f32 v139, v118, v119\n\t"
        "v_cvt_pk_bf16_f32 v140, v120, v121\n\t"
        "v_cvt_pk_bf16_f32 v141, v122, v123\n\t"
        "v_cvt_pk_bf16_f32 v142, v124, v125\n\t"
        "v_cvt_pk_bf16_f32 v143, v126, v127\n\t"
        "v_mfma_f32_32x32x16_bf16 v[112:127], v[196:199], v[148:151], 0\n\t"
        "buffer_load_dwordx4 v[182:185], %3, %7, %0 offen sc1\n\t"
        "v_exp_f32 v96, v96\n\t"
        "v_exp_f32 v97, v97\n\t"
        "v_exp_f32 v98, v98\n\t"
        "v_exp_f32 v99, v99\n\t"
        "v_exp_f32 v100, v100\n\t"
        "v_mfma_f32_32x32x16_bf16 v[80:95], v[188:191], v[136:139], v[80:95]\n\t"
        "v_exp_f32 v101, v101\n\t"
        "v_exp_f32 v102, v102\n\t"
        "v_exp_f32 v103, v103\n\t"
        "v_exp_f32 v104, v104\n\t"
        "v_exp_f32 v105, v105\n\t"
        "v_mfma_f32_32x32x8_bf16 v[112:127], v[200:201], v[162:163], v[112:127]\n\t"
        "v_exp_f32 v106, v106\n\t"
        "v_exp_f32 v107, v107\n\t"
        "v_exp_f32 v108, v108\n\t"
        "v_exp_f32 v109, v109\n\t"
        "v_exp_f32 v110, v110\n\t"
        "v_exp_f32 v111, v111\n\t"
        "v_mfma_f32_32x32x16_bf16 v[80:95], v[192:195], v[140:143], v[80:95]\n\t"
        "v_cvt_pk_bf16_f32 v128, v96, v97\n\t"
        "v_cvt_pk_bf16_f32 v129, v98, v99\n\t"
        "v_cvt_pk_bf16_f32 v130, v100, v101\n\t"
        "v_cvt_pk_bf16_f32 v131, v102, v103\n\t"
        "v_cvt_pk_bf16_f32 v132, v104, v105\n\t"
        "v_cvt_pk_bf16_f32 v133, v106, v107\n\t"
        "v_cvt_pk_bf16_f32 v134, v108, v109\n\t"
        "v_cvt_pk_bf16_f32 v135, v110, v111\n\t"
        "v_mfma_f32_32x32x16_bf16 v[96:111], v[196:199], v[152:155], 0\n\t"
        "buffer_load_dwordx2 v[186:187], %4, %7, %0 offen sc1\n\t"
        "v_exp_f32 v112, v112\n\t"
        "v_exp_f32 v113, v113\n\t"
        "v_exp_f32 v114, v114\n\t"
        "v_exp_f32 v115, v115\n\t"
        "v_exp_f32 v116, v116\n\t"
        "v_mfma_f32_32x32x16_bf16 v[32:47], v[202:205], v[128:131], v[32:47]\n\t"
        "v_exp_f32 v117, v117\n\t"
        "v_exp_f32 v118, v118\n\t"
        "v_exp_f32 v119, v119\n\t"
        "v_exp_f32 v120, v120\n\t"
        "v_exp_f32 v121, v121\n\t"
        "v_mfma_f32_32x32x8_bf16 v[96:111], v[200:201], v[164:165], v[96:111]\n\t"
        "v_exp_f32 v122, v122\n\t"
        "v_exp_f32 v123, v123\n\t"
        "v_exp_f32 v124, v124\n\t"
        "v_exp_f32 v125, v125\n\t"
        "v_exp_f32 v126, v126\n\t"
        "v_exp_f32 v127, v127\n\t"
        "v_mfma_f32_32x32x16_bf16 v[32:47], v[206:209], v[132:135], v[32:47]\n\t"
        "v_cvt_pk_bf16_f32 v136, v112, v113\n\t"
        "v_cvt_pk_bf16_f32 v137, v114, v115\n\t"
        "v_cvt_pk_bf16_f32 v138, v116, v117\n\t"
        "v_cvt_pk_bf16_f32 v139, v118, v119\n\t"
        "v_cvt_pk_bf16_f32 v140, v120, v121\n\t"
        "v_cvt_pk_bf16_f32 v141, v122, v123\n\t"
        "v_cvt_pk_bf16_f32 v142, v124, v125\n\t"
        "v_cvt_pk_bf16_f32 v143, v126, v127\n\t"
        "v_mfma_f32_32x32x16_bf16 v[112:127], v[196:199], v[156:159], 0\n\t"
        "s_mov_b64 exec, %9\n\t"
        "buffer_load_dwordx4 v[188:191], %5, %7, %0 offen sc1\n\t"
        "buffer_load_dwordx4 v[192:195], %5, %7, %0 offen offset:1280 sc1\n\t"
        "s_mov_b64 exec, %15\n\t"
        "buffer_load_dwordx4 %12, %14, %2 offen lds\n\t"
        "s_mov_b64 exec, -1\n\t"
        "v_exp_f32 v96, v96\n\t"
        "v_exp_f32 v97, v97\n\t"
        "v_exp_f32 v98, v98\n\t"
        "v_exp_f32 v99, v99\n\t"
        "v_exp_f32 v100, v100\n\t"
        "v_mfma_f32_32x32x16_bf16 v[48:63], v[202:205], v[136:139], v[48:63]\n\t"
        "v_exp_f32 v101, v101\n\t"
        "v_exp_f32 v102, v102\n\t"
        "v_exp_f32 v103, v103\n\t"
        "v_exp_f32 v104, v104\n\t"
        "v_exp_f32 v105, v105\n\t"
        "v_mfma_f32_32x32x8_bf16 v[112:127], v[200:201], v[166:167], v[112:127]\n\t"
        "v_exp_f32 v106, v106\n\t"
        "v_exp_f32 v107, v107\n\t"
        "v_exp_f32 v108, v108\n\t"
        "v_exp_f32 v109, v109\n\t"
        "v_exp_f32 v110, v110\n\t"
        "v_exp_f32 v111, v111\n\t"
        "v_mfma_f32_32x32x16_bf16 v[48:63], v[206:209], v[140:143], v[48:63]\n\t"
        "v_cvt_pk_bf16_f32 v128, v96, v97\n\t"
        "v_cvt_pk_bf16_f32 v129, v98, v99\n\t"
        "v_cvt_pk_bf16_f32 v130, v100, v101\n\t"
        "v_cvt_pk_bf16_f32 v131, v102, v103\n\t"
        "v_cvt_pk_bf16_f32 v132, v104, v105\n\t"
        "v_cvt_pk_bf16_f32 v133, v106, v107\n\t"
        "v_cvt_pk_bf16_f32 v134, v108, v109\n\t"
        "v_cvt_pk_bf16_f32 v135, v110, v111\n\t"
        "s_waitcnt vmcnt(11)\n\t"
        "v_mfma_f32_32x32x16_bf16 v[96:111], v[210:213], v[144:147], 0\n\t"
        "s_add_u32 %0, %0, 5120\n\t"
        "s_add_u32 m0, m0, 800\n\t"
        "s_add_u32 %2, %2, 6400\n\t"
        "v_exp_f32 v112, v112\n\t"
        "v_exp_f32 v113, v113\n\t"
        "v_exp_f32 v114, v114\n\t"
        "v_exp_f32 v115, v115\n\t"
        "v_exp_f32 v116, v116\n\t"
        "v_mfma_f32_32x32x16_bf16 v[64:79], v[202:205], v[128:131], v[64:79]\n\t"
        "v_exp_f32 v117, v117\n\t"
        "v_exp_f32 v118, v118\n\t"
        "v_exp_f32 v119, v119\n\t"
        "v_exp_f32 v120, v120\n\t"
        "v_exp_f32 v121, v121\n\t"
        "v_mfma_f32_32x32x8_bf16 v[96:111], v[214:215], v[160:161], v[96:111]\n\t"
        "v_exp_f32 v122, v122\n\t"
        "v_exp_f32 v123, v123\n\t"
        "v_exp_f32 v124, v124\n\t"
        "v_exp_f32 v125, v125\n\t"
        "v_exp_f32 v126, v126\n\t"
        "v_exp_f32 v127, v127\n\t"
        "v_mfma_f32_32x32x16_bf16 v[64:79], v[206:209], v[132:135], v[64:79]\n\t"
        "v_cvt_pk_bf16_f32 v136, v112, v113\n\t"
        "v_cvt_pk_bf16_f32 v137, v114, v115\n\t"
        "v_cvt_pk_bf16_f32 v138, v116, v117\n\t"
        "v_cvt_pk_bf16_f32 v139, v118, v119\n\t"
        "v_cvt_pk_bf16_f32 v140, v120, v121\n\t"
        "v_cvt_pk_bf16_f32 v141, v122, v123\n\t"
        "v_cvt_pk_bf16_f32 v142, v124, v125\n\t"
        "v_cvt_pk_bf16_f32 v143, v126, v127\n\t"
        "v_mfma_f32_32x32x16_bf16 v[112:127], v[210:213], v[148:151], 0\n\t"
        "buffer_load_dwordx4 v[196:199], %3, %7, %0 offen sc1\n\t"
        "v_exp_f32 v96, v96\n\t"
        "v_exp_f32 v97, v97\n\t"
        "v_exp_f32 v98, v98\n\t"
        "v_exp_f32 v99, v99\n\t"
        "v_exp_f32 v100, v100\n\t"
        "v_mfma_f32_32x32x16_bf16 v[80:95], v[202:205], v[136:139], v[80:95]\n\t"
        "v_exp_f32 v101, v101\n\t"
        "v_exp_f32 v102, v102\n\t"
        "v_exp_f32 v103, v103\n\t"
        "v_exp_f32 v104, v104\n\t"
        "v_exp_f32 v105, v105\n\t"
        "v_mfma_f32_32x32x8_bf16 v[112:127], v[214:215], v[162:163], v[112:127]\n\t"
        "v_exp_f32 v106, v106\n\t"
        "v_exp_f32 v107, v107\n\t"
        "v_exp_f32 v108, v108\n\t"
        "v_exp_f32 v109, v109\n\t"
        "v_exp_f32 v110, v110\n\t"
        "v_exp_f32 v111, v111\n\t"
        "v_mfma_f32_32x32x16_bf16 v[80:95], v[206:209], v[140:143], v[80:95]\n\t"
        "v_cvt_pk_bf16_f32 v128, v96, v97\n\t"
        "v_cvt_pk_bf16_f32 v129, v98, v99\n\t"
        "v_cvt_pk_bf16_f32 v130, v100, v101\n\t"
        "v_cvt_pk_bf16_f32 v131, v102, v103\n\t"
        "v_cvt_pk_bf16_f32 v132, v104, v105\n\t"
        "v_cvt_pk_bf16_f32 v133, v106, v107\n\t"
        "v_cvt_pk_bf16_f32 v134, v108, v109\n\t"
        "v_cvt_pk_bf16_f32 v135, v110, v111\n\t"
        "v_mfma_f32_32x32x16_bf16 v[96:111], v[210:213], v[152:155], 0\n\t"
        "buffer_load_dwordx2 v[200:201], %4, %7, %0 offen sc1\n\t"
        "v_exp_f32 v112, v112\n\t"
        "v_exp_f32 v113, v113\n\t"
        "v_exp_f32 v114, v114\n\t"
        "v_exp_f32 v115, v115\n\t"
        "v_exp_f32 v116, v116\n\t"
        "v_mfma_f32_32x32x16_bf16 v[32:47], v[216:219], v[128:131], v[32:47]\n\t"
        "v_exp_f32 v117, v117\n\t"
        "v_exp_f32 v118, v118\n\t"
        "v_exp_f32 v119, v119\n\t"
        "v_exp_f32 v120, v120\n\t"
        "v_exp_f32 v121, v121\n\t"
        "v_mfma_f32_32x32x8_bf16 v[96:111], v[214:215], v[164:165], v[96:111]\n\t"
        "v_exp_f32 v122, v122\n\t"
        "v_exp_f32 v123, v123\n\t"
        "v_exp_f32 v124, v124\n\t"
        "v_exp_f32 v125, v125\n\t"
        "v_exp_f32 v126, v126\n\t"
        "v_exp_f32 v127, v127\n\t"
        "v_mfma_f32_32x32x16_bf16 v[32:47], v[220:223], v[132:135], v[32:47]\n\t"
        "v_cvt_pk_bf16_f32 v136, v112, v113\n\t"
        "v_cvt_pk_bf16_f32 v137, v114, v115\n\t"
        "v_cvt_pk_bf16_f32 v138, v116, v117\n\t"
        "v_cvt_pk_bf16_f32 v139, v118, v119\n\t"
        "v_cvt_pk_bf16_f32 v140, v120, v121\n\t"
        "v_cvt_pk_bf16_f32 v141, v122, v123\n\t"
        "v_cvt_pk_bf16_f32 v142, v124, v125\n\t"
        "v_cvt_pk_bf16_f32 v143, v126, v127\n\t"
        "v_mfma_f32_32x32x16_bf16 v[112:127], v[210:213], v[156:159], 0\n\t"
        "s_mov_b64 exec, %9\n\t"
        "buffer_load_dwordx4 v[202:205], %5, %7, %0 offen sc1\n\t"
        "buffer_load_dwordx4 v[206:209], %5, %7, %0 offen offset:1280 sc1\n\t"
        "s_mov_b64 exec, %15\n\t"
        "buffer_load_dwordx4 %12, %14, %2 offen lds\n\t"
        "s_mov_b64 exec, -1\n\t"
        "v_exp_f32 v96, v96\n\t"
        "v_exp_f32 v97, v97\n\t"
        "v_exp_f32 v98, v98\n\t"
        "v_exp_f32 v99, v99\n\t"
        "v_exp_f32 v100, v100\n\t"
        "v_mfma_f32_32x32x16_bf16 v[48:63], v[216:219], v[136:139], v[48:63]\n\t"
        "v_exp_f32 v101, v101\n\t"
        "v_exp_f32 v102, v102\n\t"
        "v_exp_f32 v103, v103\n\t"
        "v_exp_f32 v104, v104\n\t"
        "v_exp_f32 v105, v105\n\t"
        "v_mfma_f32_32x32x8_bf16 v[112:127], v[214:215], v[166:167], v[112:127]\n\t"
        "v_exp_f32 v106, v106\n\t"
        "v_exp_f32 v107, v107\n\t"
        "v_exp_f32 v108, v108\n\t"
        "v_exp_f32 v109, v109\n\t"
        "v_exp_f32 v110, v110\n\t"
        "v_exp_f32 v111, v111\n\t"
        "v_mfma_f32_32x32x16_bf16 v[48:63], v[220:223], v[140:143], v[48:63]\n\t"
        "v_cvt_pk_bf16_f32 v128, v96, v97\n\t"
        "v_cvt_pk_bf16_f32 v129, v98, v99\n\t"
        "v_cvt_pk_bf16_f32 v130, v100, v101\n\t"
        "v_cvt_pk_bf16_f32 v131, v102, v103\n\t"
        "v_cvt_pk_bf16_f32 v132, v104, v105\n\t"
        "v_cvt_pk_bf16_f32 v133, v106, v107\n\t"
        "v_cvt_pk_bf16_f32 v134, v108, v109\n\t"
        "v_cvt_pk_bf16_f32 v135, v110, v111\n\t"
        "s_waitcnt vmcnt(11)\n\t"
        "v_mfma_f32_32x32x16_bf16 v[96:111], v[168:171], v[144:147], 0\n\t"
        "s_add_u32 %0, %0, 5120\n\t"
        "s_add_u32 m0, m0, 800\n\t"
        "s_add_u32 %2, %2, 32000\n\t"
        "s_sub_u32 %1, %1, 1\n\t"
        "s_cmp_lg_u32 %1, 0\n\t"
        "v_exp_f32 v112, v112\n\t"
        "v_exp_f32 v113, v113\n\t"
        "v_exp_f32 v114, v114\n\t"
        "v_exp_f32 v115, v115\n\t"
        "v_exp_f32 v116, v116\n\t"
        "v_mfma_f32_32x32x16_bf16 v[64:79], v[216:219], v[128:131], v[64:79]\n\t"
        "v_exp_f32 v117, v117\n\t"
        "v_exp_f32 v118, v118\n\t"
        "v_exp_f32 v119, v119\n\t"
        "v_exp_f32 v120, v120\n\t"
        "v_exp_f32 v121, v121\n\t"
        "v_mfma_f32_32x32x8_bf16 v[96:111], v[172:173], v[160:161], v[96:111]\n\t"
        "v_exp_f32 v122, v122\n\t"
        "v_exp_f32 v123, v123\n\t"
        "v_exp_f32 v124, v124\n\t"
        "v_exp_f32 v125, v125\n\t"
        "v_exp_f32 v126, v126\n\t"
        "v_exp_f32 v127, v127\n\t"
        "v_mfma_f32_32x32x16_bf16 v[64:79], v[220:223], v[132:135], v[64:79]\n\t"
        "v_cvt_pk_bf16_f32 v136, v112, v113\n\t"
        "v_cvt_pk_bf16_f32 v137, v114, v115\n\t"
        "v_cvt_pk_bf16_f32 v138, v116, v117\n\t"
        "v_cvt_pk_bf16_f32 v139, v118, v119\n\t"
        "v_cvt_pk_bf16_f32 v140, v120, v121\n\t"
        "v_cvt_pk_bf16_f32 v141, v122, v123\n\t"
        "v_cvt_pk_bf16_f32 v142, v124, v125\n\t"
        "v_cvt_pk_bf16_f32 v143, v126, v127\n\t"
        "s_cbranch_scc1 LOOP%=\n\t"
        "ds_read_b32 v128, %11\n\t"
        "s_mov_b32 %1, 0x100000\n\t"
        "v_mfma_f32_32x32x16_bf16 v[80:95], v[216:219], v[136:139], v[80:95]\n\t"
        "v_mfma_f32_32x32x16_bf16 v[80:95], v[220:223], v[140:143], v[80:95]\n\t"
        "WAIT%=:\n\t"
        "s_waitcnt lgkmcnt(0)\n\t"
        "v_readfirstlane_b32 %0, v128\n\t"
        "s_sub_u32 %1, %1, 1\n\t"
        "s_cbranch_scc1 GO%=\n\t"
        "s_cmp_ge_u32 %0, 4\n\t"
        "s_cbranch_scc1 GO%=\n\t"
        "s_sleep 2\n\t"
        "ds_read_b32 v128, %11\n\t"
        "s_branch WAIT%=\n\t"
        "GO%=:\n\t"
        "ds_write_b128 %6, v[32:35] offset:0\n\t"
        "ds_write_b128 %6, v[36:39] offset:1024\n\t"
        "ds_write_b128 %6, v[40:43] offset:2048\n\t"
        "ds_write_b128 %6, v[48:51] offset:3072\n\t"
        "ds_write_b128 %6, v[52:55] offset:4096\n\t"
        "ds_write_b128 %6, v[56:59] offset:5120\n\t"
        "ds_write_b128 %6, v[64:67] offset:6144\n\t"
        "ds_write_b128 %6, v[68:71] offset:7168\n\t"
        "ds_write_b128 %6, v[72:75] offset:8192\n\t"
        "s_nop 7\n\t"
        "ds_write_b128 %6, v[80:83] offset:9216\n\t"
        "ds_write_b128 %6, v[84:87] offset:10240\n\t"
        "ds_write_b128 %6, v[88:91] offset:11264\n\t"
        "s_waitcnt vmcnt(1) lgkmcnt(0)\n\t"
        : "=&s"(so), "=&s"(cnt), "=&s"(sw)
        : "v"(vo16), "v"(vo4), "v"(voV), "v"(la), "s"(srdK), "s"(srdQ), "s"(0x000FFFFF000FFFFFull), "s"(0x00000000FFFFFFFFull), "v"(ldsword), "v"(voW), "v"(ones), "s"(srdW), "s"(0x0003FFFFFFFFFFFFull), "s"(wol),
          "s"(X), "v"(fo), "s"(ws), "s"(tag), "v"(gow)
        : "memory", "scc", "vcc", "v32", "v33", "v34", "v35", "v36", "v37", "v38", "v39", "v40", "v41", "v42", "v43", "v44", "v45", "v46", "v47", "v48", "v49", "v50", "v51", "v52", "v53", "v54", "v55", "v56", "v57", "v58", "v59", "v60", "v61", "v62", "v63", "v64", "v65", "v66", "v67", "v68", "v69", "v70", "v71", "v72", "v73", "v74", "v75", "v76", "v77", "v78", "v79", "v80", "v81", "v82", "v83", "v84", "v85", "v86", "v87", "v88", "v89", "v90", "v91", "v92", "v93", "v94", "v95", "v96", "v97", "v98", "v99", "v100", "v101", "v102", "v103", "v104", "v105", "v106", "v107", "v108", "v109", "v110", "v111", "v112", "v113", "v114", "v115", "v116", "v117", "v118", "v119", "v120", "v121", "v122", "v123", "v124", "v125", "v126", "v127", "v128", "v129", "v130", "v131", "v132", "v133", "v134", "v135", "v136", "v137", "v138", "v139", "v140", "v141", "v142", "v143", "v144", "v145", "v146", "v147", "v148", "v149", "v150", "v151", "v152", "v153", "v154", "v155", "v156", "v157", "v158", "v159", "v160", "v161", "v162", "v163", "v164", "v165", "v166", "v167", "v168", "v169", "v170", "v171", "v172", "v173", "v174", "v175", "v176", "v177", "v178", "v179", "v180", "v181", "v182", "v183", "v184", "v185", "v186", "v187", "v188", "v189", "v190", "v191", "v192", "v193", "v194", "v195", "v196", "v197", "v198", "v199", "v200", "v201", "v202", "v203", "v204", "v205", "v206", "v207", "v208", "v209", "v210", "v211", "v212", "v213", "v214", "v215", "v216", "v217", "v218", "v219", "v220", "v221", "v222", "v223");
}

DEV bool attn_finish(lds_ptr lds, int l, int X, int n, int i, int p, rsrc_t rs) {
    const int hi = l >> 5;
    f32x4 o[3];
    f32x4 t[3][4];
#pragma unroll
    for (int rr = 0; rr < 3; ++rr)
#pragma unroll
        for (int kq = 0; kq < 4; ++kq) t[rr][kq] = *(const __attribute__((address_space(3))) f32x4*)(lds + L_RED + ((kq * 4 + n) * 3 + (rr + 2) % 3) * 1024 + l * 16);
    if (FB) __builtin_amdgcn_sched_barrier(0);
#pragma unroll
    for (int rr = 0; rr < 3; ++rr) o[(rr + 2) % 3] = (t[rr][0] + t[rr][1]) + (t[rr][2] + t[rr][3]);
    const auto sw = __builtin_amdgcn_permlane32_swap(__float_as_uint(o[2][0]), __float_as_uint(o[2][0]), false, false);
    const float lsum = __uint_as_float(sw[1]);
    const float rl = 1.0f / lsum;
    float chk = 0.f;
#pragma unroll
    for (int rg = 0; rg < 3; ++rg)
#pragma unroll
        for (int e = 0; e < 4; ++e) chk += fabsf(o[rg][e]);
    const unsigned ob = OFF_O + (i >> 1) * SS + (X * 16 + 8 * (i & 1) + 2 * n + ((l >> 4) & 1)) * 10240;
    st16(rs, ob + (2 * p + hi) * 256 + (l & 15) * 16,
         (u32x4){cvtpk(o[0][0] * rl, o[0][1] * rl), cvtpk(o[0][2] * rl, o[0][3] * rl), cvtpk(o[1][0] * rl, o[1][1] * rl), cvtpk(o[1][2] * rl, o[1][3] * rl)});
    if (!hi) st8(rs, ob + 8192 + p * 128 + (l & 15) * 8, (u32x2){cvtpk(o[2][0] * rl, o[2][1] * rl), cvtpk(o[2][2] * rl, o[2][3] * rl)});
    return !(lsum > 9.765625e-4f && lsum < 1.0e12f && chk < 1.0e30f);
}

__global__ __launch_bounds__(512, 2) void fused(const u16* __restrict__ x, const u16* __restrict__ W, const u16* __restrict__ wo,
                                                const u16* __restrict__ bk, const u16* __restrict__ bv, const u16* __restrict__ shk,
                                                const u16* __restrict__ ssmk, const u16* __restrict__ ssmD, const u16* __restrict__ gw,
                                                char* ws, u16* __restrict__ out) {
    __shared__ __attribute__((aligned(16))) char lds_[L_BYTES];
    lds_ptr lds = (lds_ptr)lds_;
    const unsigned lds0 = (unsigned)(uintptr_t)lds_;
    const int tid = threadIdx.x, l = tid & 63, w = RFL(tid >> 6);
    const int b = blockIdx.x, xc = b & 7, wi = b >> 3;
    {
        u64 pc;
        asm volatile("s_getpc_b64 %0\n\tglobal_load_dword v255, %1, %0" : "=&s"(pc) : "v"(min((wi * 16 + (tid >> 5)) * 64, CODEPF)) : "v255");
    }
    asm volatile("" ::"s"(x), "s"(W), "s"(wo), "s"(bk), "s"(bv), "s"(shk), "s"(ssmk), "s"(ssmD), "s"(gw), "s"(ws), "s"(out));
    u64 ep;
    asm volatile("s_load_dwordx2 %0, %1, %2" : "=s"(ep) : "s"(ws), "s"(OFF_EP));
    __builtin_amdgcn_sched_barrier(0);
    const rsrc_t rs = __builtin_amdgcn_make_buffer_rsrc(ws, 0, WS_USED, 0x00020000);
    const int gu = (xc & 1) * 30 + wi, gj = gu >> 2, gi = 4 * (xc >> 1) + (gu & 3);
    const bool hasg = wi < 30;
    const int p = 2 * xc + (wi & 1), i = wi >> 1;
    const int tt = 4 * xc + (wi & 3), nb = wi >> 2;
    const int X = w >> 2, kq = w & 3;
    const int c16 = l & 15, G = l >> 4;
    const int chl = 5 * (wi >> 1) + 2 * (wi & 1), ch0 = 80 * xc + chl;
    lds_u32 misc = (lds_u32)(lds + L_MISC);
    unsigned gen = 0;
    u64 tag;
    TS_INIT();
    TS(0);

    if (X == 0) {
        prep_load<true>(lds0 + L_CV + w * PR_W, bk, bv, shk, l, 64 * tt + 16 * w, 64 * nb);
        if (w == 3) prep_load<false>(lds0 + L_CV - PR_W, bk, bv, shk, l, 64 * tt, 512 + 16 * nb);
        if (tid < 16) misc[tid] = 0;
        tag = mktag(ep);
        TS(5);
        if (w == 3) asm volatile("s_waitcnt vmcnt(5)" ::: "memory");
        else asm volatile("s_waitcnt vmcnt(0)" ::: "memory");
        TS(7);
        {
            lds_ptr s = lds + L_CV + w * PR_W, d = lds + L_CV + PR_T + l * 144 + w * 32;
            int zl = (tt | w) == 0 ? 64 : 0;
#pragma unroll 1
            for (int n = w == 3 ? 2 : 1; n > 0; --n) {
                prep_math(s, d, l, l < zl);
                s = lds + L_CV - PR_W;
                d = s + c16 * 144 + G * 32;
                zl = tt == 0 ? 16 : 0;
                asm volatile("s_waitcnt vmcnt(0)" ::: "memory");
            }
        }
        const int tl = (l >> 3) * 144 + (l & 7) * 16;
        if (w == 3) {
            const unsigned uo = prep_dst(l, tt, 512 + 16 * nb);
            st16(rs, uo, __builtin_bit_cast(u32x4, ld128(lds + L_CV - PR_W + tl)));
            st16(rs, uo + 8 * 4096, __builtin_bit_cast(u32x4, ld128(lds + L_CV - PR_W + tl + 8 * 144)));
        }
        TS(1);
        const unsigned f2off = OFF_F2 + (nb * 32 + tt) * FS2, uo = prep_dst(l, tt, 64 * nb + 16 * w);
        if (!hasg) {
            wait_lgkm_bar();
            st16(rs, uo, __builtin_bit_cast(u32x4, ld128(lds + L_CV + PR_T + 16 * 144 * w + tl)));
            st16(rs, uo + 8 * 4096, __builtin_bit_cast(u32x4, ld128(lds + L_CV + PR_T + 16 * 144 * w + tl + 8 * 144)));
            if (arrive(lds, C_F2, l) == 3) put_flag(ws, f2off, tag, l);
            const int gx = 2 * xc + wi - 30;
            const u16* xr = x + (512 * (gx >> 2) + 16 * (gx & 3) + 64 * (l >> 3) + 4 * kq) * 640 + 8 * (l & 7);
            u32x4 gf[10], xa[4][10];
#pragma unroll
            for (int s = 0; s < 10; ++s) gf[s] = *(const u32x4*)(gw + 64 * s + 8 * (l & 7));
#pragma unroll
            for (int n = 0; n < 4; ++n)
#pragma unroll
                for (int s = 0; s < 10; ++s) xa[n][s] = *(const u32x4*)(xr + 640 * n + 64 * s);
            __builtin_amdgcn_sched_barrier(0);
            float z = 0.f;
#pragma unroll
            for (int n = 0; n < 4; ++n) {
                float a0 = 0.f, a1 = 0.f;
#pragma unroll
                for (int s = 0; s < 10; ++s)
#pragma unroll
                    for (int e = 0; e < 4; e += 2) {
                        a0 = dot2(xa[n][s][e], gf[s][e], a0);
                        a1 = dot2(xa[n][s][e + 1], gf[s][e + 1], a1);
                    }
                a0 += a1;
                a0 += __shfl_xor(a0, 1); a0 += __shfl_xor(a0, 2); a0 += __shfl_xor(a0, 4);
                z = (l & 3) == n ? a0 : z;
            }
            if (!(l & 4)) st4(rs, OFF_G + (128 * gx + 32 * kq + 8 * (l & 3) + (l >> 3)) * 4, __float_as_uint(1.0f / (1.0f + __expf(-z))));
            if (arrive(lds, C_F1, l) == 3) put_flag(ws, F1(gx, 15), tag, l);
            if (b == 255 && w == 0) {
#pragma unroll 1
                for (int n = 0; n < 4; ++n) poll(ws, OFF_F2 + (n * 64 + l) * FS2, tag);
                put_flag(ws, OFF_EP, tag, l);
            }
        } else {
            if (gj >= 10) gemm_a<true>(lds, lds0, l, w, gi, gj, ws, rs, tag, f2off, uo);
            else gemm_a<false>(lds, lds0, l, w, gi, gj, ws, rs, tag, f2off, uo);
            TS(3);
            if (arrive(lds, C_F1, l) == 3) put_flag(ws, F1(gi, gj), tag, l);
        }
        TS(4);
        lds_wait<1>(lds, C_F1, 4);
    } else {
        auto stage_h = [&]() {
#pragma unroll
            for (int ci = 0; ci < 3; ++ci) {
                setm0(lds0 + L_CV + ci * CV_CH + 64 + kq * 1024);
                gldsnt((const char*)ssmk + (size_t)(ch0 + ci) * 4096 + kq * 1024, l * 16);
            }
        };
        const u64 dq = *(const __attribute__((address_space(4))) u64 __attribute__((aligned(4)))*)(uintptr_t)(ssmD + (ch0 & ~1)) >> (16 * (ch0 & 1));
        const int cf = ch0 + ((l >> 2) & 2);
        const unsigned f2o = OFF_F2 + ((cf < 512 ? cf >> 6 : (cf >> 4) - 32) * 32 + 8 * kq + (l & 7)) * FS2, uoff = OFF_U + xc * SS + chl * 4096 + kq * 1024 + l * 16;
        u32x4 uv[3];
        bool got = false;
        if (hasg) {
            unsigned xvo[4], wvo[4];
#pragma unroll
            for (int n = 0; n < 4; ++n) {
                const int lam = 32 * kq + 8 * n + (l >> 3), t16 = lam & 15;
                const int tl = gj >= 10 ? (t16 & 12) | ((t16 & 1) << 1) | ((t16 >> 1) & 1) : t16;
                xvo[n] = (512 * (gi >> 2) + 16 * (gi & 3) + 64 * (lam >> 4) + tl) * 1280 + (((l & 7) ^ (lam >> 4)) * 16) - 1024 * n;
                const int k = 16 * kq + 4 * n + (l >> 4), r3 = (k & 3) | (((k >> 3) & 1) << 2);
                wvo[n] = k * 3840 + gj * 256 + (((l & 15) ^ (2 * r3)) * 16) - 1024 * n;
            }
            int c = (((gu & 3) * 5 + (gj & 7) * 5 / 2) % 20) >> 1;
            const u32x4 srx = {(unsigned)(uintptr_t)x, (unsigned)((uintptr_t)x >> 32), 2048u * 1280u, 0x20000u};
            const u32x4 srw = {(unsigned)(uintptr_t)W, (unsigned)((uintptr_t)W >> 32), 640u * 3840u, 0x20000u};
            const u32x4 srd = {(unsigned)(uintptr_t)ws, (unsigned)((uintptr_t)ws >> 32), WS_USED, 0x20000u};
            const unsigned sl = lds0 + kq * 4096;
            unsigned cx = c * 128, cw = c * 245760;
            u64 fl;
            int tbi = 0;
            u64 tb[20];
            TS(12);
            setm0(sl + 0);
            blds<0>(srx, xvo[0], cx);
            blds<1024>(srx, xvo[1], cx);
            blds<2048>(srx, xvo[2], cx);
            blds<3072>(srx, xvo[3], cx);
            setm0(sl + 16384);
            blds<0>(srw, wvo[0], cw);
            blds<1024>(srw, wvo[1], cw);
            blds<2048>(srw, wvo[2], cw);
            blds<3072>(srw, wvo[3], cw);
            cx = cx == 1152 ? 0 : cx + 128; cw = cx * 1920;
            setm0(sl + 32768);
            blds<0>(srx, xvo[0], cx);
            blds<1024>(srx, xvo[1], cx);
            blds<2048>(srx, xvo[2], cx);
            blds<3072>(srx, xvo[3], cx);
            setm0(sl + 49152);
            blds<0>(srw, wvo[0], cw);
            blds<1024>(srw, wvo[1], cw);
            blds<2048>(srw, wvo[2], cw);
            blds<3072>(srw, wvo[3], cw);
            cx = cx == 1152 ? 0 : cx + 128; cw = cx * 1920;
            setm0(sl + 65536);
            blds<0>(srx, xvo[0], cx);
            blds<1024>(srx, xvo[1], cx);
            blds<2048>(srx, xvo[2], cx);
            blds<3072>(srx, xvo[3], cx);
            setm0(sl + 81920);
            blds<0>(srw, wvo[0], cw);
            blds<1024>(srw, wvo[1], cw);
            blds<2048>(srw, wvo[2], cw);
            blds<3072>(srw, wvo[3], cw);
            TS(14);
            WVB(16);
            tag = mktag(ep);
            cx = cx == 1152 ? 0 : cx + 128; cw = cx * 1920;
            setm0(sl + 98304);
            blds<0>(srx, xvo[0], cx);
            blds<1024>(srx, xvo[1], cx);
            blds<2048>(srx, xvo[2], cx);
            blds<3072>(srx, xvo[3], cx);
            setm0(sl + 114688);
            blds<0>(srw, wvo[0], cw);
            blds<1024>(srw, wvo[1], cw);
            blds<2048>(srw, wvo[2], cw);
            blds<3072>(srw, wvo[3], cw);
            WVB(16);
            cx = cx == 1152 ? 0 : cx + 128; cw = cx * 1920;
            setm0(sl + 0);
            blds<0>(srx, xvo[0], cx);
            blds<1024>(srx, xvo[1], cx);
            blds<2048>(srx, xvo[2], cx);
            blds<3072>(srx, xvo[3], cx);
            setm0(sl + 16384);
            blds<0>(srw, wvo[0], cw);
            blds<1024>(srw, wvo[1], cw);
            blds<2048>(srw, wvo[2], cw);
            blds<3072>(srw, wvo[3], cw);
            WVB(16);
            cx = cx == 1152 ? 0 : cx + 128; cw = cx * 1920;
            setm0(sl + 32768);
            blds<0>(srx, xvo[0], cx);
            blds<1024>(srx, xvo[1], cx);
            blds<2048>(srx, xvo[2], cx);
            blds<3072>(srx, xvo[3], cx);
            setm0(sl + 49152);
            blds<0>(srw, wvo[0], cw);
            blds<1024>(srw, wvo[1], cw);
            blds<2048>(srw, wvo[2], cw);
            blds<3072>(srw, wvo[3], cw);
            WVB(16);
            cx = cx == 1152 ? 0 : cx + 128; cw = cx * 1920;
            setm0(sl + 65536);
            blds<0>(srx, xvo[0], cx);
            blds<1024>(srx, xvo[1], cx);
            blds<2048>(srx, xvo[2], cx);
            blds<3072>(srx, xvo[3], cx);
            setm0(sl + 81920);
            blds<0>(srw, wvo[0], cw);
            blds<1024>(srw, wvo[1], cw);
            blds<2048>(srw, wvo[2], cw);
            blds<3072>(srw, wvo[3], cw);
            WVB(16);
            cx = cx == 1152 ? 0 : cx + 128; cw = cx * 1920;
            setm0(sl + 98304);
            blds<0>(srx, xvo[0], cx);
            blds<1024>(srx, xvo[1], cx);
            blds<2048>(srx, xvo[2], cx);
            blds<3072>(srx, xvo[3], cx);
            setm0(sl + 114688);
            blds<0>(srw, wvo[0], cw);
            blds<1024>(srw, wvo[1], cw);
            blds<2048>(srw, wvo[2], cw);
            blds<3072>(srw, wvo[3], cw);
            WVB(16);
            cx = cx == 1152 ? 0 : cx + 128; cw = cx * 1920;
            setm0(sl + 0);
            blds<0>(srx, xvo[0], cx);
            blds<1024>(srx, xvo[1], cx);
            blds<2048>(srx, xvo[2], cx);
            blds<3072>(srx, xvo[3], cx);
            setm0(sl + 16384);
            blds<0>(srw, wvo[0], cw);
            blds<1024>(srw, wvo[1], cw);
            blds<2048>(srw, wvo[2], cw);
            blds<3072>(srw, wvo[3], cw);
            WVB(16);
            cx = cx == 1152 ? 0 : cx + 128; cw = cx * 1920;
            setm0(sl + 32768);
            blds<0>(srx, xvo[0], cx);
            blds<1024>(srx, xvo[1], cx);
            blds<2048>(srx, xvo[2], cx);
            blds<3072>(srx, xvo[3], cx);
            setm0(sl + 49152);
            blds<0>(srw, wvo[0], cw);
            blds<1024>(srw, wvo[1], cw);
            blds<2048>(srw, wvo[2], cw);
            blds<3072>(srw, wvo[3], cw);
            asm volatile("global_load_dwordx2 %0, %1, %2 sc1" : "=v"(fl) : "v"(f2o), "s"(ws) : "memory");
            stage_h();
            WVB(20);
            WVB(12);
            WVB(3);
            asm volatile("" : "+v"(fl));
            got = __all(fl == tag);
            asm volatile("buffer_load_dwordx4 %0, %3, %4, 0 offen sc1\n\tbuffer_load_dwordx4 %1, %3, %4, %5 offen sc1\n\tbuffer_load_dwordx4 %2, %3, %4, %6 offen sc1"
                         : "=&v"(uv[0]), "=&v"(uv[1]), "=&v"(uv[2]) : "v"(got ? uoff : 0xC0000000u), "s"(srd), "s"(4096), "s"(8192) : "memory");
            TS(3);
            asm volatile("s_waitcnt vmcnt(0)" : "+v"(uv[0]), "+v"(uv[1]), "+v"(uv[2])::"memory");
        } else {
            tag = mktag(ep);
            if (HPF == 2) wait_lgkm_bar();
            if (HPF) {
                const char* hp = (const char*)ssmk + (80 * xc + 40 * (wi & 1) + 10 * kq) * 4096;
                setm0(lds0);
#pragma unroll
                for (int n = 0; n < 5; ++n) asm volatile("global_load_lds_dword %0, %1" ::"v"(l * 128), "s"(hp + n * 8192) : "memory");
            }
            if (HPF != 2) wait_lgkm_bar();
            lds_wait<8>(lds, C_F2, 4);
            stage_h();
        }
        if (kq < 3 && l < 36) {
            const int off = l < 4 ? l * 16 : (l < 20 ? CV_H + (l - 4) * 16 : CV_H + CV_UH + (l - 20) * 16);
            *(__attribute__((address_space(3))) u32x4*)(lds + L_CV + kq * CV_CH + off) = (u32x4){0, 0, 0, 0};
        }
        if (!got) {
            poll(ws, f2o, tag);
#pragma unroll
            for (int ci = 0; ci < 3; ++ci) uv[ci] = ld16(rs, uoff + ci * 4096);
            TS(13);
        }
        TS(4);
        {
            const int oct = 64 * kq + l, uo = L_CV + CV_H + ((oct >> 1) & 1) * CV_UH + 256 + (oct >> 2) * 32 + (oct & 1) * 16;
#pragma unroll
            for (int ci = 0; ci < 3; ++ci) *(__attribute__((address_space(3))) u32x4*)(lds + uo + ci * CV_CH) = uv[ci];
        }
        asm volatile("s_waitcnt vmcnt(0)" ::: "memory");
        group_bar(lds, 1, l, gen);
        if (PRT >= 0) __builtin_amdgcn_s_setprio(2);
        TS(5);
        {
            unsigned lst;
            if (!(wi & 1)) lst = kq == 0 ? 0xF13 : kq == 1 ? 0xF57 : kq == 2 ? 0xF04B : 0xF826;
            else lst = kq == 0 ? 0xF57 : kq == 1 ? 0xF9B : kq == 2 ? 0xF6A : 0xF4812;
            const int gh = G & 1, q = c16 >> 2, pp = l & 3, hh = l >> 5;
            const unsigned hb = lds0 + L_CV + 2 * (16 + 16 * gh + 4 * pp - 4 * q + 32 * hh);
            const unsigned ub = lds0 + L_CV + CV_H + (2 + 4 * gh + pp - hh) * 32 + 8 * q - 448;
#pragma unroll 1
            for (; (lst & 15) != 15; lst >>= 4) {
                const int ci = (lst & 15) >> 2, prr = lst & 3, d = chl + ci;
                const unsigned Dv = (unsigned)(dq >> (16 * ci)) & 0xffffu, Dl = (l & 4) ? Dv << 16 : Dv;
                u32x4 fD;
#pragma unroll
                for (int e = 0; e < 4; ++e) fD[e] = (l & 0x3b) == 8 * e ? Dl : 0u;
                u32x4 pk;
                unsigned z1, z2, z3;
                conv_asm(pk, z1, z2, z3, prr, ub + ci * CV_CH + 512 * prr, hb + ci * CV_CH, __builtin_bit_cast(bf16x8, fD));
                st16(rs, OFF_YY + xc * SS + d * 4096 + prr * 1024 + l * 16, pk);
                st16(rs, (prr < 3 && l == 63) ? OFF_CARRY + xc * SS + ((prr + 1) * 80 + d) * 16 : 0xC0000000u, (u32x4){z1, z2, z3, 0u});
            }
            TS(15);
            if ((FORCE_SLOW || !LFY) && arrive(lds, C_Y, l) == 3) put_flag(ws, OFF_FY + (xc * 32 + wi) * FSY, tag, l);
        }
        TS(6);
    }

    const int q = c16 >> 2, pp = l & 3;
    const int rho = 64 * (tt & 1) + 16 * kq + c16, go = tt >> 1;
    const int t = 512 * (go >> 2) + 16 * (go & 3) + 64 * (rho & 7) + (rho >> 3);
    u32x4 yv[3];
    u32x2 xv[5];
    unsigned tch;
    const rsrc_t ro = __builtin_amdgcn_make_buffer_rsrc(out, 0, 2048 * 1280, 0x00020000);
    float g;
    auto tail_x = [&]() {
#pragma unroll
        for (int nt = 0; nt < 5; ++nt) xv[nt] = *(const u32x2*)(x + t * 640 + 80 * nb + 16 * nt + 4 * G);
    };
    auto tail_t = [&]() {
        tch = __builtin_amdgcn_raw_buffer_load_b32(ro, t * 1280 + 160 * nb + 32 * G, 0, 17);
    };
    auto tail_y = [&]() {
        poll(ws, l < 32 ? OFF_FY + (nb * 32 + l) * FSY : F1(go, 15), tag);
#pragma unroll
        for (int n = 0; n < 3; ++n) {
            const int idx = 64 * n + (n < 2 ? l : l & 31);
            yv[n] = ld16(rs, OFF_YY + nb * SS + (idx >> 1) * 4096 + (64 * tt + 16 * kq) * 2 + (idx & 1) * 16);
        }
        g = __uint_as_float(ld4(rs, OFF_G + (128 * go + rho) * 4));
    };
    {
        const unsigned qb = p * PS + OFF_Q + 4 * i * QT, kvb = p * PS + 16 * kq * KVT;
        const char* wob = (const char*)wo + (160 * kq + 20 * X) * 1280 + 160 * nb;
        const unsigned wol = lds0 + X * 51200 + kq * 12800;
        const int cw = X ? C_DONE : C_Y;
        const int jl = (40 * p) >> 7, jh = (40 * p + 39) >> 7, js = (l & 1) ? jh : jl;
        const unsigned f1o = F1(l < 16 ? 4 * kq + (l >> 2) : i, l < 16 ? 5 + 5 * ((l >> 1) & 1) + js : js);
        attn_fast(lds0 + L_RED + kq * 12288, lds0 + L_MISC + cw * 4, l, X, ws + qb, ws + kvb, wob, wol, X ? OFF_FY + (xc * 32 + wi) * FSY : f1o, ws, tag, lds0 + L_MISC + (C_GO + kq) * 4,
                  ws + (200u << 20) + 262144 + (b * 512 + tid) * 4, lds0 + L_MISC + 64 + (w * 16 + 8) * 8);
        TS(9);
        if (EARLY) {
            asm volatile("s_waitcnt lgkmcnt(0)" ::: "memory");
            if (l == 0) __hip_atomic_fetch_add(misc + C_GB + X, 1u, RLX_WG);
            gen += 4;
            if (!X) { tail_y(); tail_x(); if (EARLY == 2) tail_t(); }
            lds_wait<1>(lds, C_GB + X, gen);
        } else group_bar(lds, X, l, gen);
        TS(10);
        const unsigned fo = FO(X, i, p);
        if (__any(attn_finish(lds, l, X, kq, i, p, rs)) && !EXPT) misc[C_BAD + X] = 1;
        else if (EDN && !X && l == 0) __hip_atomic_fetch_add(misc + C_DONE, 1u, RLX_WG);
        gen += 4;
        const bool last = arrive(lds, C_GB + X, l) == gen - 1;
        if (!last) lds_wait<1>(lds, C_GB + X, gen);
        if (RFL(__hip_atomic_load(misc + C_BAD + X, RLX_WG)) == 0) {
            if (last) { put_flag(ws, fo, tag, l); if (!X) misc[C_DONE] = 4; }
        } else {
            attn_slow(lds, l, X, kq, rs, qb, kvb, gen);
            group_bar(lds, X, l, gen);
            attn_finish(lds, l, X, kq, i, p, rs);
            gen += 4;
            if (arrive(lds, C_GB + X, l) == gen - 1) { put_flag(ws, fo, tag, l); if (!X) misc[C_DONE] = 4; }
        }
        TS(11);
    }
    if (X) {
        TB_DUMP();
        TS_DUMP();
        return;
    }

    if (EARLY != 2) tail_t();
    if (!EARLY) { tail_y(); tail_x(); }
    float pre[5][4];
    {
        lds_ptr bn = lds + L_BN + kq * 2560;
#pragma unroll
        for (int n = 0; n < 3; ++n) *(__attribute__((address_space(3))) u32x4*)(bn + n * 1024 + (n < 2 ? l : l & 31) * 16) = yv[n];
        asm volatile("s_waitcnt lgkmcnt(0)" ::: "memory");
        float yf[5][4];
#pragma unroll
        for (int nt = 0; nt < 5; ++nt) {
            const u32x2 y2 = __builtin_bit_cast(u32x2, ldtr(bn + (16 * nt + 4 * G + q) * 32 + 8 * pp));
            yf[nt][0] = bflo(y2[0]); yf[nt][1] = bfhi(y2[0]); yf[nt][2] = bflo(y2[1]); yf[nt][3] = bfhi(y2[1]);
        }
        if ((tt & 7) == 0 && tt > 0 && kq < 2) {
            const int e = 2 * kq + (c16 >> 3);
            const unsigned co = ((c16 & 7) == 0 && e < 3) ? OFF_CARRY + nb * SS + ((tt >> 3) * 80 + 4 * G) * 16 + e * 4 : 0xC0000000u;
#pragma unroll
            for (int nt = 0; nt < 5; ++nt)
#pragma unroll
                for (int j = 0; j < 4; ++j) yf[nt][j] += __uint_as_float(ld4(rs, co + (16 * nt + j) * 16));
        }
        const float g1 = 1.0f - g;
#pragma unroll
        for (int nt = 0; nt < 5; ++nt) {
            pre[nt][0] = g1 * bflo(xv[nt][0]) * yf[nt][0]; pre[nt][1] = g1 * bfhi(xv[nt][0]) * yf[nt][1];
            pre[nt][2] = g1 * bflo(xv[nt][1]) * yf[nt][2]; pre[nt][3] = g1 * bfhi(xv[nt][1]) * yf[nt][3];
        }
    }
    f32x4 acc[5];
#pragma unroll
    for (int nt = 0; nt < 5; ++nt) acc[nt] = (f32x4){0.f, 0.f, 0.f, 0.f};
    lds_cptr wl1 = lds + (20 * (G >> 1) + 4 * (G & 1) + q) * 160 + pp * 8, wl2 = lds + (20 * (4 * (G >> 1) + (G & 1)) + 16 + q) * 160 + pp * 8;
    constexpr int PD = 6;
#pragma unroll
    for (int hf = 0; hf < 2; ++hf) {
        bf16x8 wf[PD][5];
        auto ldw = [&](int s) {
#pragma unroll
            for (int nt = 0; nt < 5; ++nt) wf[s % PD][nt] = s < 8 ? ldtr2(wl1 + hf * 51200 + s * 6400 + nt * 32, wl1 + hf * 51200 + s * 6400 + nt * 32 + 1280)
                                                                 : ldtr2(wl2 + hf * 51200 + (s - 8) * 25600 + nt * 32, wl2 + hf * 51200 + (s - 8) * 25600 + nt * 32 + 6400);
        };
        if (hf) { TS(12); lds_wait<8>(lds, C_GB + 1, 8); if (EV1) lds_wait<1>(lds, C_GB + 1, 12); }
#pragma unroll
        for (int s = 0; s < PD; ++s) ldw(s);
#define POLL do { if (hf) poll<SL1>(ws, FO(1, go, c16), tag); else poll<SL0>(ws, FO(0, go, c16), tag); } while (0)
        if (HR) {
            if (kq == 0) { poll<SL1>(ws, FO(hf, go, c16), tag); misc[C_FO + hf] = 1; }
            else lds_wait<HR>(lds, C_FO + hf, 1);
        } else if (ONEP & (1 << hf)) {
            if (kq == 0) { POLL; misc[C_FO + hf] = 1; }
            else if (hf) lds_wait<1>(lds, C_FO + 1, 1);
            else lds_wait<4>(lds, C_FO, 1);
        } else POLL;
        if (hf) TS(13);
        const unsigned ob = OFF_O + xc * SS + (hf * 16 + 4 * (wi & 3) + kq) * 10240;
        u32x4 of[10];
#pragma unroll
        for (int s = 0; s < 8; ++s) of[s] = ld16(rs, ob + s * 1024 + l * 16);
#pragma unroll
        for (int s = 0; s < 2; ++s) {
            const unsigned qo = ob + 8192 + (8 * s + 4 * (G >> 1) + (G & 1)) * 128 + c16 * 8;
            const u32x2 a = ld8(rs, qo), c = ld8(rs, qo + 256);
            of[8 + s] = (u32x4){a[0], a[1], c[0], c[1]};
        }
        __builtin_amdgcn_sched_barrier(0);
#pragma unroll
        for (int s = 0; s < 10; ++s) {
#pragma unroll
            for (int nt = 0; nt < 5; ++nt) acc[nt] = MFMA16(wf[s % PD][nt], __builtin_bit_cast(bf16x8, of[s]), acc[nt]);
            if (s + PD < 10) ldw(s + PD);
        }
    }
    asm volatile("" ::"v"(tch));
    TS(14);
    u32x2 ov[5];
#pragma unroll
    for (int nt = 0; nt < 5; ++nt)
        ov[nt] = (u32x2){cvtpk(g * acc[nt][0] + pre[nt][0], g * acc[nt][1] + pre[nt][1]), cvtpk(g * acc[nt][2] + pre[nt][2], g * acc[nt][3] + pre[nt][3])};
#pragma unroll
    for (int n = 0; n < 2; ++n) {
        const auto s0 = __builtin_amdgcn_permlane16_swap(ov[2 * n][0], ov[2 * n + 1][0], false, false);
        const auto s1 = __builtin_amdgcn_permlane16_swap(ov[2 * n][1], ov[2 * n + 1][1], false, false);
        __builtin_amdgcn_raw_buffer_store_b128((u32x4){s0[0], s1[0], s0[1], s1[1]}, ro, t * 1280 + 160 * nb + 64 * n + 32 * (G & 1) + 16 * (G >> 1), 0, OST);
    }
    __builtin_amdgcn_raw_buffer_store_b64(ov[4], ro, t * 1280 + 160 * nb + 128 + 8 * G, 0, OST);
    TS(15);
    TS_DUMP();
}

extern "C" void kernel_launch(void* const* d_in, const int* in_sizes, int n_in, void* d_out, int out_size, void* d_ws, size_t ws_size,
                              hipStream_t stream) {
    fused<<<256, 512, 0, stream>>>((const u16*)d_in[0], (const u16*)d_in[1], (const u16*)d_in[2], (const u16*)d_in[4], (const u16*)d_in[5],
                                   (const u16*)d_in[6], (const u16*)d_in[7], (const u16*)d_in[8], (const u16*)d_in[9], (char*)d_ws, (u16*)d_out);
}
```
